```python
import torch

def _softmax(x, axis=-1):
    return torch.softmax(x, dim=axis)
def _gelu(x):
    return torch.nn.functional.gelu(x, approximate="tanh")
def _silu(x):
    return torch.nn.functional.silu(x)
def _sigmoid(x):
    return torch.sigmoid(x)
def _layernorm(x, axis=-1, eps=1e-5):
    m = x.mean(dim=axis, keepdim=True)
    v = x.var(dim=axis, keepdim=True, unbiased=False)
    return (x - m) / torch.sqrt(v + eps)
def _scatter_add(tgt, idx, src, axis=0):
    return tgt.clone().index_add_(axis, idx, src)
def _segment_sum(src, seg, n):
    out = torch.zeros((n,) + src.shape[1:], dtype=src.dtype)
    return out.index_add_(0, seg, src)
def _pad(x, pw):
    flat = tuple(v for pair in reversed(pw) for v in pair)
    return torch.nn.functional.pad(x, flat)

kernel_name = 'standalone__mixer_channel_mlp__prompt'

def setup_inputs():

    _w = (lambda a: (lambda t: t.to(torch.bfloat16) if t.is_floating_point() else t)(torch.as_tensor(a)))
    B, S, D, F = 1, 2048, 512, 1792
    return {
        "x": _w((lambda *s: torch.randn(*s, dtype=torch.bfloat16) * 0.05)(B, S, D)),
        "w1": _w((lambda *s: torch.randn(*s, dtype=torch.bfloat16) * 0.8)(D, F) * (D ** -0.5)),
        "b1": _w((lambda *s: torch.randn(*s, dtype=torch.bfloat16) * 0.4)(F)),
        "w2": _w((lambda *s: torch.randn(*s, dtype=torch.bfloat16) * 0.05)(F, D) * (F ** -0.5)),
        "b2": _w((lambda *s: torch.randn(*s, dtype=torch.bfloat16) * 0.05)(D)),
    }

def reference(x, w1, b1, w2, b2):
    y = _layernorm(x, axis=-1)
    h = _gelu(torch.einsum("bsd,df->bsf", y, w1) + b1)
    return torch.einsum("bsf,fd->bsd", h, w2) + b2
```

```cpp
#include <hip/hip_runtime.h>
#include <hip/hip_bf16.h>
typedef __attribute__((ext_vector_type(8))) short bf16x8;
typedef __attribute__((ext_vector_type(4))) float f32x4;
typedef unsigned short u16;
constexpr int M=2048, D=512, F=1792;

__device__ __forceinline__ float bf2f(u16 a){ return __uint_as_float(((unsigned)a)<<16); }
__device__ __forceinline__ u16 f2bf(float f){ unsigned u=__float_as_uint(f); u += 0x7fffu + ((u>>16)&1u); return (u16)(u>>16); }

__global__ void ln_k(const u16* x, u16* y){
  int row = blockIdx.x; int t = threadIdx.x;
  float v[8]; float s=0;
  for(int j=0;j<8;j++){ v[j]=bf2f(x[row*D+t*8+j]); s+=v[j]; }
  for(int o=32;o;o>>=1) s+=__shfl_xor(s,o);
  float m=s/D; float q=0;
  for(int j=0;j<8;j++){ float d=v[j]-m; q+=d*d; }
  for(int o=32;o;o>>=1) q+=__shfl_xor(q,o);
  float r=rsqrtf(q/D+1e-5f);
  for(int j=0;j<8;j++) y[row*D+t*8+j]=f2bf((v[j]-m)*r);
}
template<int K,int N,bool GELU>
__global__ void gemm_k(const u16* A,const u16* B,const u16* bias,u16* C){
  int lane=threadIdx.x&63; int w=threadIdx.x>>6;
  int tm=blockIdx.y*16, tn=(blockIdx.x*4+w)*16;
  f32x4 acc={0,0,0,0};
  int r=lane&15,g=lane>>4;
  for(int k=0;k<K;k+=32){
    bf16x8 a=*(const bf16x8*)(A+(tm+r)*K+k+g*8);
    bf16x8 b;
    for(int j=0;j<8;j++) b[j]=B[(k+g*8+j)*N+tn+r];
    acc=__builtin_amdgcn_mfma_f32_16x16x32_bf16(a,b,acc,0,0,0);
  }
  float bb=bf2f(bias[tn+r]);
  for(int j=0;j<4;j++){
    float v=acc[j]+bb;
    if(GELU){ float u=0.7978845608028654f*(v+0.044715f*v*v*v); v=0.5f*v*(1.f+tanhf(u)); }
    C[(tm+g*4+j)*N+tn+r]=f2bf(v);
  }
}
extern "C" void kernel_launch(void* const* d_in, const int* in_sizes, int n_in,
                              void* d_out, int out_size, void* d_ws, size_t ws_size,
                              hipStream_t stream) {
  const u16* x=(const u16*)d_in[0]; const u16* w1=(const u16*)d_in[1]; const u16* b1=(const u16*)d_in[2];
  const u16* w2=(const u16*)d_in[3]; const u16* b2=(const u16*)d_in[4];
  u16* y=(u16*)d_ws; u16* h=y+M*D;
  ln_k<<<M,64,0,stream>>>(x,y);
  gemm_k<D,F,true><<<dim3(F/64,M/16),256,0,stream>>>(y,w1,b1,h);
  gemm_k<F,D,false><<<dim3(D/64,M/16),256,0,stream>>>(h,w2,b2,(u16*)d_out);
}
```

```cpp
#include <hip/hip_runtime.h>
#include <hip/hip_bf16.h>
typedef __attribute__((ext_vector_type(8))) short bf16x8;
typedef __attribute__((ext_vector_type(4))) float f32x4;
typedef unsigned short u16;
constexpr int M=2048, D=512, F=1792;

__device__ __forceinline__ float bf2f(u16 a){ return __uint_as_float(((unsigned)a)<<16); }
__device__ __forceinline__ u16 f2bf(float f){ unsigned u=__float_as_uint(f); u += 0x7fffu + ((u>>16)&1u); return (u16)(u>>16); }

__global__ void ln_k(const u16* x, u16* y){
  int row = blockIdx.x; int t = threadIdx.x;
  float v[8]; float s=0;
  for(int j=0;j<8;j++){ v[j]=bf2f(x[row*D+t*8+j]); s+=v[j]; }
  for(int o=32;o;o>>=1) s+=__shfl_xor(s,o);
  float m=s/D; float q=0;
  for(int j=0;j<8;j++){ float d=v[j]-m; q+=d*d; }
  for(int o=32;o;o>>=1) q+=__shfl_xor(q,o);
  float r=rsqrtf(q/D+1e-5f);
  for(int j=0;j<8;j++) y[row*D+t*8+j]=f2bf((v[j]-m)*r);
}
template<int K,int N,bool GELU>
__global__ void gemm_k(const u16* A,const u16* B,const u16* bias,u16* C){
  int lane=threadIdx.x&63; int w=threadIdx.x>>6;
  int tm=blockIdx.y*16, tn=(blockIdx.x*4+w)*16;
  f32x4 acc={0,0,0,0};
  int r=lane&15,g=lane>>4;
  for(int k=0;k<K;k+=32){
    bf16x8 a=*(const bf16x8*)(A+(tm+r)*K+k+g*8);
    bf16x8 b;
    for(int j=0;j<8;j++) b[j]=B[(k+g*8+j)*N+tn+r];
    acc=__builtin_amdgcn_mfma_f32_16x16x32_bf16(a,b,acc,0,0,0);
  }
  float bb=bf2f(bias[tn+r]);
  for(int j=0;j<4;j++){
    float v=acc[j]+bb;
    if(GELU){ float u=0.7978845608028654f*(v+0.044715f*v*v*v); v=0.5f*v*(1.f+tanhf(u)); }
    C[(tm+g*4+j)*N+tn+r]=f2bf(v);
  }
}

__global__ void probe_k(const unsigned* p, int n, int stride_dw, unsigned* sink){
  unsigned off=0; unsigned acc=0;
  for(int i=0;i<n;i++){
    unsigned v;
    const unsigned* q=p+off;
    asm volatile("global_load_dword %0, %1, off\n\ts_waitcnt vmcnt(0)":"=v"(v):"v"(q):"memory");
    acc+=v;
    unsigned z; asm volatile("v_and_b32 %0, 0, %1":"=v"(z):"v"(v));
    off+=stride_dw+z;
  }
  if(acc==0x12345) sink[0]=acc;
}
extern "C" void kernel_launch(void* const* d_in, const int* in_sizes, int n_in,
                              void* d_out, int out_size, void* d_ws, size_t ws_size,
                              hipStream_t stream) {
  const u16* x=(const u16*)d_in[0]; const u16* w1=(const u16*)d_in[1]; const u16* b1=(const u16*)d_in[2];
  const u16* w2=(const u16*)d_in[3]; const u16* b2=(const u16*)d_in[4];
  u16* y=(u16*)d_ws; u16* h=y+M*D;
  probe_k<<<1,64,0,stream>>>((const unsigned*)x,0,1056,(unsigned*)d_ws+60000000);
  ln_k<<<M,64,0,stream>>>(x,y);
  gemm_k<D,F,true><<<dim3(F/64,M/16),256,0,stream>>>(y,w1,b1,h);
  gemm_k<F,D,false><<<dim3(D/64,M/16),256,0,stream>>>(h,w2,b2,(u16*)d_out);
}
```

```cpp
#include <hip/hip_runtime.h>
#include <hip/hip_bf16.h>
typedef __attribute__((ext_vector_type(8))) short bf16x8;
typedef __attribute__((ext_vector_type(4))) float f32x4;
typedef unsigned short u16;
constexpr int M=2048, D=512, F=1792;

__device__ __forceinline__ float bf2f(u16 a){ return __uint_as_float(((unsigned)a)<<16); }
__device__ __forceinline__ u16 f2bf(float f){ unsigned u=__float_as_uint(f); u += 0x7fffu + ((u>>16)&1u); return (u16)(u>>16); }

__global__ void ln_k(const u16* x, u16* y){
  int row = blockIdx.x; int t = threadIdx.x;
  float v[8]; float s=0;
  for(int j=0;j<8;j++){ v[j]=bf2f(x[row*D+t*8+j]); s+=v[j]; }
  for(int o=32;o;o>>=1) s+=__shfl_xor(s,o);
  float m=s/D; float q=0;
  for(int j=0;j<8;j++){ float d=v[j]-m; q+=d*d; }
  for(int o=32;o;o>>=1) q+=__shfl_xor(q,o);
  float r=rsqrtf(q/D+1e-5f);
  for(int j=0;j<8;j++) y[row*D+t*8+j]=f2bf((v[j]-m)*r);
}
template<int K,int N,bool GELU>
__global__ void gemm_k(const u16* A,const u16* B,const u16* bias,u16* C){
  int lane=threadIdx.x&63; int w=threadIdx.x>>6;
  int tm=blockIdx.y*16, tn=(blockIdx.x*4+w)*16;
  f32x4 acc={0,0,0,0};
  int r=lane&15,g=lane>>4;
  for(int k=0;k<K;k+=32){
    bf16x8 a=*(const bf16x8*)(A+(tm+r)*K+k+g*8);
    bf16x8 b;
    for(int j=0;j<8;j++) b[j]=B[(k+g*8+j)*N+tn+r];
    acc=__builtin_amdgcn_mfma_f32_16x16x32_bf16(a,b,acc,0,0,0);
  }
  float bb=bf2f(bias[tn+r]);
  for(int j=0;j<4;j++){
    float v=acc[j]+bb;
    if(GELU){ float u=0.7978845608028654f*(v+0.044715f*v*v*v); v=0.5f*v*(1.f+tanhf(u)); }
    C[(tm+g*4+j)*N+tn+r]=f2bf(v);
  }
}

typedef unsigned v4u __attribute__((ext_vector_type(4)));
__global__ __launch_bounds__(256) void probe_k(const char* src, int kb){
  const char* p=src+(size_t)blockIdx.x*kb*1024+threadIdx.x*128;
  for(int i=0;i<kb/32;i++){
    unsigned v;
    asm volatile("global_load_dword %0, %1, off ":"=v"(v):"v"(p):"memory");
    p+=32768;
  }
  asm volatile("s_waitcnt vmcnt(0)":::"memory");
}
extern "C" void kernel_launch(void* const* d_in, const int* in_sizes, int n_in,
                              void* d_out, int out_size, void* d_ws, size_t ws_size,
                              hipStream_t stream) {
  const u16* x=(const u16*)d_in[0]; const u16* w1=(const u16*)d_in[1]; const u16* b1=(const u16*)d_in[2];
  const u16* w2=(const u16*)d_in[3]; const u16* b2=(const u16*)d_in[4];
  u16* y=(u16*)d_ws; u16* h=y+M*D;
  probe_k<<<256,256,0,stream>>>((const char*)d_ws+(64<<20),0);
  ln_k<<<M,64,0,stream>>>(x,y);
  gemm_k<D,F,true><<<dim3(F/64,M/16),256,0,stream>>>(y,w1,b1,h);
  gemm_k<F,D,false><<<dim3(D/64,M/16),256,0,stream>>>(h,w2,b2,(u16*)d_out);
}
```

```cpp
#include <hip/hip_runtime.h>

typedef unsigned short u16;
typedef unsigned v4u __attribute__((ext_vector_type(4)));
typedef unsigned v2u __attribute__((ext_vector_type(2)));
typedef short bf16x8 __attribute__((ext_vector_type(8)));
typedef short s16x4 __attribute__((ext_vector_type(4)));
typedef float f32x16 __attribute__((ext_vector_type(16)));
typedef float f32x4 __attribute__((ext_vector_type(4)));
typedef float f32x2 __attribute__((ext_vector_type(2)));
#define LAS __attribute__((address_space(3)))
#define DI __device__ __forceinline__

#define DIST 2
#define ROTMODE 1

constexpr int M_ = 2048, D_ = 512, F_ = 1792;
constexpr unsigned WS_EPOCH = 0;
constexpr unsigned WS_FLAGS = 4096;
constexpr unsigned WS_STATS = 32768;
constexpr unsigned WS_H = 65536;
constexpr unsigned WS_BYTES = WS_H + 64u * 112u * 1024u;
constexpr unsigned WS_STAMP = 16u << 20;
constexpr unsigned TAG_STEP = 0x9E3779B9u;
constexpr int AUX_SC1 = 16;
constexpr unsigned SPIN_CAP = 1u << 16;
constexpr unsigned REG = 32768;

#define STAMP(i)

DI unsigned rfl(unsigned v) { return __builtin_amdgcn_readfirstlane(v); }
DI float bf_lo(unsigned d) { return __uint_as_float(d << 16); }
DI float bf_hi(unsigned d) { return __uint_as_float(d & 0xffff0000u); }
DI unsigned cvt_pk(float lo, float hi) {
  typedef __bf16 bf2 __attribute__((ext_vector_type(2)));
  f32x2 v = {lo, hi};
  return __builtin_bit_cast(unsigned, __builtin_convertvector(v, bf2));
}
DI f32x16 mfma32(v4u a, v4u b, f32x16 c) {
  return __builtin_amdgcn_mfma_f32_32x32x16_bf16(__builtin_bit_cast(bf16x8, a), __builtin_bit_cast(bf16x8, b), c, 0, 0, 0);
}
DI f32x4 mfma16(v4u a, v4u b, f32x4 c) {
  return __builtin_amdgcn_mfma_f32_16x16x32_bf16(__builtin_bit_cast(bf16x8, a), __builtin_bit_cast(bf16x8, b), c, 0, 0, 0);
}
DI v2u tr_read(LAS const char* p) {
  return __builtin_bit_cast(v2u, __builtin_amdgcn_ds_read_tr16_b64_v4i16((LAS s16x4*)p));
}
DI v4u tr_frag(LAS const char* p, int step) {
  v2u a = tr_read(p), b = tr_read(p + step);
  return v4u{a.x, a.y, b.x, b.y};
}
#define BARRIER() do { asm volatile("s_waitcnt lgkmcnt(0)\n\ts_barrier" ::: "memory"); } while (0)
#define WAITV(n) asm volatile("s_waitcnt vmcnt(%0)" :: "i"(n) : "memory")

template <int LS, int OS>
DI void dma4(__amdgpu_buffer_rsrc_t r, unsigned v, unsigned l, unsigned o) {
  asm volatile(
      "s_mov_b32 m0, %[l]\n\ts_nop 0\n\tbuffer_load_dwordx4 %[v], %[r], %[o] offen lds\n\t"
      "s_add_u32 m0, m0, %[ls]\n\ts_add_u32 %[o], %[o], %[os]\n\tbuffer_load_dwordx4 %[v], %[r], %[o] offen lds\n\t"
      "s_add_u32 m0, m0, %[ls]\n\ts_add_u32 %[o], %[o], %[os]\n\tbuffer_load_dwordx4 %[v], %[r], %[o] offen lds\n\t"
      "s_add_u32 m0, m0, %[ls]\n\ts_add_u32 %[o], %[o], %[os]\n\tbuffer_load_dwordx4 %[v], %[r], %[o] offen lds"
      : [o] "+s"(o)
      : [v] "v"(v), [r] "s"(r), [l] "s"(l), [ls] "i"(LS), [os] "i"(OS)
      : "memory", "scc");
}

__global__ __launch_bounds__(512, 2) void mixer_mlp(const u16* __restrict__ x, const u16* __restrict__ w1, const u16* __restrict__ b1,
                                                    const u16* __restrict__ w2, const u16* __restrict__ b2, u16* __restrict__ out,
                                                    char* ws) {
  __shared__ __attribute__((aligned(16))) char lds_[163840];
  LAS char* lds = (LAS char*)lds_;
  const unsigned lds0 = (unsigned)(size_t)lds;
  STAMP(0);
  const int lane = threadIdx.x & 63;
  const int wave = rfl(threadIdx.x >> 6);
  const int wg = blockIdx.x, xcd = wg & 7, j = wg >> 3;
  const bool busy = j < 28;
  const int jd = (j * 37) >> 8;
  const int p1 = 4 * (xcd >> 1) + jd, c1 = 7 * (xcd & 1) + (j - 7 * jd);
  const int p2 = 2 * xcd + (j >> 4), rh = (j >> 3) & 1, n2 = (j + 4 * (xcd & 1)) & 7;
  const __amdgpu_buffer_rsrc_t rsW = __builtin_amdgcn_make_buffer_rsrc((void*)ws, 0, WS_BYTES, 0x00020000);
  const int h = lane >> 5, r32 = lane & 31;

  if (wave < 4) {
    const int lw = wave;
    const __amdgpu_buffer_rsrc_t rsX = __builtin_amdgcn_make_buffer_rsrc((void*)x, 0, M_ * D_ * 2, 0x00020000);
    const __amdgpu_buffer_rsrc_t rs1 = __builtin_amdgcn_make_buffer_rsrc((void*)w1, 0, D_ * F_ * 2, 0x00020000);
    const __amdgpu_buffer_rsrc_t rs2 = __builtin_amdgcn_make_buffer_rsrc((void*)w2, 0, D_ * F_ * 2, 0x00020000);
    const unsigned vx = (lane >> 3) * 1024 + (((lane & 7) ^ (4 * (lw & 1) + (lane >> 4))) * 16);
    const unsigned v1 = (lane >> 4) * (F_ * 2) + (((lane & 15) ^ ((lane >> 4) << 2)) * 16);
    const unsigned v2 = (lane >> 3) * 1024 + (((lane & 7) ^ (((lane >> 4) & 1) << 2)) * 16);
    const unsigned ox = (p1 * 128 + 8 * lw) * 1024, o1 = 4 * lw * (F_ * 2) + c1 * 256;
    int rot = 0;
    if (ROTMODE == 1) rot = (p1 + c1) & 7;
    if (ROTMODE == 2) rot = ((p1 + c1) & 1) * 4;
    if (ROTMODE == 3) rot = ((p1 + c1) & 3) * 2;
    const int rot2 = (n2 & 1) * 14;
    auto issue_stage = [&](int s) {
      const unsigned ksl = (s + rot) & 7, l = lds0 + (s & 3) * REG + lw * 1024;
      dma4<4096, 32768>(rsX, vx, l, ox + ksl * 128);
      dma4<4096, 16 * F_ * 2>(rs1, v1, l + 16384, o1 + ksl * 64 * (F_ * 2));
    };
    auto issue_group = [&](int g, int reg) {
      const unsigned l = lds0 + reg * REG + lw * 8192;
#pragma unroll
      for (int u = 0; u < 2; ++u) {
        int i = 4 * g + 2 * u + rot2;
        if (i >= 28) i -= 28;
        dma4<1024, 8192>(rs2, v2, l + u * 4096, (28 * lw + i) * 16384 + n2 * 128);
      }
    };
    if (busy) {
#pragma unroll
      for (int q = 0; q < DIST; ++q) issue_stage(q);
#pragma unroll 1
      for (int s = 0; s < 8 - DIST; ++s) {
        WAITV(8 * (DIST - 1));
        BARRIER();
        if (s == 0) STAMP(1);
        issue_stage(s + DIST);
      }
    } else {
      BARRIER();
    }
    const unsigned tag2 = rfl(*(LAS volatile unsigned*)(lds + 4 * REG + lw * 8192));
    asm volatile("s_waitcnt lgkmcnt(0)" ::: "memory");
    if (busy) {
      if (DIST == 3) { WAITV(16); BARRIER(); issue_group(0, 4); WAITV(16); BARRIER(); issue_group(1, 0); WAITV(16); STAMP(2); BARRIER(); }
      if (DIST == 2) { WAITV(8); BARRIER(); issue_group(0, 4); WAITV(8); STAMP(2); BARRIER(); issue_group(1, 0); }
    } else {
      issue_group(0, 4); issue_group(1, 0);
    }
    issue_group(2, 1);
    issue_group(3, 2);
    const int q4 = (lane & 15) >> 2, pp = lane & 3, g1 = (lane >> 4) & 1;
    LAS const char* tb0 = lds + lw * 8192 + (4 * h + q4) * 128 + (((q4 >> 1) & 1) * 64) + 32 * g1 + 8 * pp;
    LAS const char* tb1 = lds + lw * 8192 + (4 * h + q4) * 128 + ((((q4 >> 1) & 1) ^ 1) * 64) + 32 * g1 + 8 * pp;
    LAS const char *tb0r2 = tb0 + 2 * REG, *tb1r2 = tb1 + 2 * REG, *tb0r4 = tb0 + 4 * REG, *tb1r4 = tb1 + 4 * REG;
    asm volatile("" : "+v"(tb0r2), "+v"(tb1r2), "+v"(tb0r4), "+v"(tb1r4));
    v4u wr[12][2];
#pragma unroll
    for (int g = 0; g < 3; ++g) {
      const int reg = g == 0 ? 4 : g - 1;
      WAITV(24);
#pragma unroll
      for (int i = 0; i < 4; ++i) {
        wr[4 * g + i][0] = tr_frag((g == 0 ? tb0r4 : tb0 + reg * REG) + i * 2048, 1024);
        wr[4 * g + i][1] = tr_frag((g == 0 ? tb1r4 : tb1 + reg * REG) + i * 2048, 1024);
      }
#pragma unroll
      for (int i = 0; i < 4; ++i) asm volatile("" : "+v"(wr[4 * g + i][0]), "+v"(wr[4 * g + i][1]));
      issue_group(4 + g, reg);
    }
    STAMP(3);
    BARRIER();
    WAITV(0);
    STAMP(4);
    {
      const unsigned fo = WS_FLAGS + (p2 * 4 + lw) * 128 + (lane < 14 ? lane : 0) * 8;
#pragma unroll 1
      for (unsigned spins = 0; spins < SPIN_CAP; ++spins) {
        const v2u f = __builtin_amdgcn_raw_buffer_load_b64(rsW, fo, 0, AUX_SC1);
        if (__all(f.x == tag2 && f.y == ~tag2)) break;
        asm volatile("s_sleep 1" ::: "memory");
      }
      __builtin_amdgcn_fence(__ATOMIC_ACQUIRE, "wavefront");
    }
    STAMP(5);
    constexpr int RD = 7;
    const unsigned vh = lane * 16;
    auto hoff = [&](int t, int i) -> unsigned {
      int ii = i + rot2;
      if (ii >= 28) ii -= 28;
      return WS_H + (unsigned)(((p2 * 4 + rh + 2 * t) * 112 + 28 * lw + ii) * 1024);
    };
    v4u hr[RD];
#pragma unroll
    for (int i = 0; i < RD; ++i) hr[i] = __builtin_amdgcn_raw_buffer_load_b128(rsW, vh, hoff(0, i), AUX_SC1);
    __builtin_amdgcn_sched_barrier(0);
#pragma unroll 1
    for (int t = 0; t < 2; ++t) {
      f32x16 acc0 = {}, acc1 = {};
      v4u an[2][2];
      const unsigned vtail = t == 0 ? vh : 0x40000000u;
      auto rdw = [&](int i, v4u (&a)[2]) {
        const int g = i >> 2;
        LAS const char* b0 = g == 3 ? tb0r2 : (g == 4 ? tb0r4 : tb0 + (g - 5) * REG);
        LAS const char* b1 = g == 3 ? tb1r2 : (g == 4 ? tb1r4 : tb1 + (g - 5) * REG);
        a[0] = tr_frag(b0 + (i & 3) * 2048, 1024);
        a[1] = tr_frag(b1 + (i & 3) * 2048, 1024);
      };
#pragma unroll
      for (int i = 0; i < 28; ++i) {
        v4u a0, a1;
        if (i < 12) { a0 = wr[i][0]; a1 = wr[i][1]; }
        else { a0 = an[i & 1][0]; a1 = an[i & 1][1]; }
        const v4u b = hr[i % RD];
        acc0 = mfma32(a0, b, acc0);
        acc1 = mfma32(a1, b, acc1);
        __builtin_amdgcn_sched_barrier(0);
        const unsigned o = (i + RD < 28) ? hoff(t, i + RD) : hoff(1, i + RD - 28);
        hr[i % RD] = __builtin_amdgcn_raw_buffer_load_b128(rsW, (i + RD < 28) ? vh : vtail, o, AUX_SC1);
        if (i + 2 >= 12 && i + 2 < 28) rdw(i + 2, an[i & 1]);
        __builtin_amdgcn_sched_barrier(0);
      }
      LAS char* pb = lds + (3 + t) * REG + lw * 8192;
#pragma unroll
      for (int g = 0; g < 4; ++g) {
        *(LAS f32x4*)(pb + g * 1024 + ((lane ^ (g << 2)) * 16)) = f32x4{acc0[4 * g], acc0[4 * g + 1], acc0[4 * g + 2], acc0[4 * g + 3]};
        *(LAS f32x4*)(pb + 4096 + g * 1024 + ((lane ^ (g << 2)) * 16)) = f32x4{acc1[4 * g], acc1[4 * g + 1], acc1[4 * g + 2], acc1[4 * g + 3]};
      }
      if (t == 0) STAMP(6); else STAMP(7);
      BARRIER();
    }
  } else {
    const int cw = wave - 4;
    const unsigned e1 = __builtin_amdgcn_raw_buffer_load_b32(rsW, 0, WS_EPOCH + p1 * 256, AUX_SC1);
    const unsigned e2 = __builtin_amdgcn_raw_buffer_load_b32(rsW, 0, WS_EPOCH + p2 * 256, AUX_SC1);
    const int srow = p2 * 128 + 8 * (j & 15) + 2 * cw + h;
    const v4u* xr = (const v4u*)(x + srow * D_);
    const v4u xa = xr[r32], xb = xr[32 + r32];
    const unsigned bias1 = busy ? b1[c1 * 128 + cw * 32 + r32] : 0;
    const v4u bias2 = *(const v4u*)(b2 + n2 * 64 + (lane & 7) * 8);
    const unsigned tag1 = rfl(e1) + TAG_STEP, tag2 = rfl(e2) + TAG_STEP;
    if (lane == 0) *(LAS volatile unsigned*)(lds + 4 * REG + cw * 8192) = tag2;
    {
      float v[16];
#pragma unroll
      for (int i = 0; i < 4; ++i) { v[2 * i] = bf_lo(xa[i]); v[2 * i + 1] = bf_hi(xa[i]); v[8 + 2 * i] = bf_lo(xb[i]); v[9 + 2 * i] = bf_hi(xb[i]); }
      float s = 0.f;
#pragma unroll
      for (int i = 0; i < 16; ++i) s += v[i];
#pragma unroll
      for (int o = 16; o; o >>= 1) s += __shfl_xor(s, o);
      const float mean = s * (1.f / D_);
      float q = 0.f;
#pragma unroll
      for (int i = 0; i < 16; ++i) { const float d = v[i] - mean; q = fmaf(d, d, q); }
#pragma unroll
      for (int o = 16; o; o >>= 1) q += __shfl_xor(q, o);
      const float sig = __builtin_sqrtf(q * (1.f / D_) + 1e-5f);
      const unsigned m1 = cvt_pk(-mean, sig);
      const unsigned m2 = cvt_pk(-mean - bf_lo(m1), sig - bf_hi(m1));
      const v4u gr = {(m1 & 0xffffu) | (m2 << 16), tag2, (m1 >> 16) | (m2 & 0xffff0000u), tag2};
      if (r32 == 0) __builtin_amdgcn_raw_buffer_store_b128(gr, rsW, WS_STATS + srow * 16, 0, AUX_SC1);
    }
    STAMP(1);
    if (busy) {
      const int q4 = (lane & 15) >> 2, pp = lane & 3, g1 = (lane >> 4) & 1;
      const int aoff = 16384 + (8 * h + q4) * 256 + ((cw ^ q4) * 64) + 32 * g1 + 8 * pp;
      int boff[4];
#pragma unroll
      for (int ks = 0; ks < 4; ++ks) boff[ks] = r32 * 128 + (((2 * ks + h) ^ ((r32 >> 1) & 7)) * 16);
      const unsigned one2 = 0x3f803f80u;
      const unsigned selv = ((((lane & 15) >> 2) ^ (lane >> 4)) & 1) ? 0u : one2;
      const v4u sel = {selv, selv, selv, selv};
      f32x16 acc[4] = {};
      f32x4 cs = {};
      v4u A, B[4], st[4];
      BARRIER();
      STAMP(2);
      auto rd = [&](int s, int ks, v4u& a, v4u (&b)[4]) {
        LAS const char* base = lds + (s & 3) * REG;
        a = tr_frag(base + aoff + ks * 4096, 1024);
#pragma unroll
        for (int t = 0; t < 4; ++t) b[t] = *(LAS const v4u*)(base + boff[ks] + t * 4096);
      };
      rd(0, 0, A, B);
#pragma unroll
      for (int s = 0; s < 8; ++s) {
        if (s == 5) {
#pragma unroll
          for (int t = 0; t < 4; ++t) st[t] = __builtin_amdgcn_raw_buffer_load_b128(rsW, WS_STATS + (p1 * 128 + t * 32 + r32) * 16, 0, AUX_SC1);
        }
#pragma unroll
        for (int ks = 0; ks < 4; ++ks) {
          v4u An, Bn[4];
          if (ks < 3) rd(s, ks + 1, An, Bn);
          else if (s < 7) { BARRIER(); rd(s + 1, 0, An, Bn); }
          cs = mfma16(sel, A, cs);
#pragma unroll
          for (int t = 0; t < 4; ++t) acc[t] = mfma32(A, B[t], acc[t]);
          A = An;
#pragma unroll
          for (int t = 0; t < 4; ++t) B[t] = Bn[t];
          __builtin_amdgcn_sched_barrier(0);
        }
      }
      STAMP(3);
#pragma unroll 1
      for (unsigned spins = 0; spins < SPIN_CAP; ++spins) {
        bool ok = true;
#pragma unroll
        for (int t = 0; t < 4; ++t) ok &= st[t].y == tag1 && st[t].w == tag1;
        if (__all(ok)) break;
        asm volatile("s_sleep 1" ::: "memory");
#pragma unroll
        for (int t = 0; t < 4; ++t) st[t] = __builtin_amdgcn_raw_buffer_load_b128(rsW, WS_STATS + (p1 * 128 + t * 32 + r32) * 16, 0, AUX_SC1);
      }
      {
        const float s0 = cs[0];
        const unsigned a1 = cvt_pk(s0, s0);
        const float r1 = s0 - bf_lo(a1);
        const unsigned a2 = cvt_pk(r1, r1);
        const float r2 = r1 - bf_lo(a2);
        const unsigned a3 = cvt_pk(r2, r2);
        const v4u ax = {h ? (bias1 | (bias1 << 16)) : a1, h ? 0u : a2, h ? 0u : a3, 0u};
#pragma unroll
        for (int t = 0; t < 4; ++t) {
          const unsigned bv = h ? st[t].z : st[t].x;
          acc[t] = mfma32(ax, v4u{bv, bv, bv, bv}, acc[t]);
        }
      }
      STAMP(4);
      constexpr float C0 = -2.3022081985f, C1 = -0.1029432397f;
#pragma unroll
      for (int t = 0; t < 4; ++t) {
        const float sig = bf_lo(st[t].z) + bf_hi(st[t].z);
        const float r = __builtin_amdgcn_rcpf(sig);
        const float k0 = C0 * r, k1 = C1 * r * r * r;
        unsigned o[8];
#pragma unroll
        for (int i = 0; i < 8; ++i) {
          const f32x2 v = {acc[t][2 * i], acc[t][2 * i + 1]};
          const f32x2 u = v * (v * v * k1 + k0);
          const f32x2 e = {__builtin_amdgcn_exp2f(u.x), __builtin_amdgcn_exp2f(u.y)};
          const f32x2 d = e * sig + sig;
          const f32x2 qq = {__builtin_amdgcn_rcpf(d.x), __builtin_amdgcn_rcpf(d.y)};
          const f32x2 g = v * qq;
          o[i] = cvt_pk(g.x, g.y);
        }
        const unsigned ho = WS_H + ((p1 * 4 + t) * 112 + 8 * c1 + 2 * cw) * 1024;
        __builtin_amdgcn_raw_buffer_store_b128(v4u{o[0], o[1], o[2], o[3]}, rsW, lane * 16 + ho, 0, AUX_SC1);
        __builtin_amdgcn_raw_buffer_store_b128(v4u{o[4], o[5], o[6], o[7]}, rsW, lane * 16 + ho + 1024, 0, AUX_SC1);
      }
      STAMP(5);
      WAITV(0);
      STAMP(6);
      {
        const int idx = 4 * c1 + cw, kq = (idx * 74) >> 10;
        if (lane == 0) __builtin_amdgcn_raw_buffer_store_b64(v2u{tag1, ~tag1}, rsW, WS_FLAGS + (p1 * 4 + kq) * 128 + (idx - 14 * kq) * 8, 0, AUX_SC1);
      }
    } else {
      BARRIER();
    }
    BARRIER();
    const int nb = (lane & 7) >> 2, g = lane & 3, tok = (lane >> 3) + 8 * cw;
#pragma unroll 1
    for (int t = 0; t < 2; ++t) {
      BARRIER();
      if (t == 0 && (j & 15) == 0 && cw == 0 && lane == 0) __builtin_amdgcn_raw_buffer_store_b32(tag2, rsW, WS_EPOCH + p2 * 256, 0, AUX_SC1);
      LAS const char* pb = lds + (3 + t) * REG + nb * 4096 + g * 1024 + ((tok ^ (g << 2)) * 16);
      f32x4 lo = {bf_lo(bias2.x), bf_hi(bias2.x), bf_lo(bias2.y), bf_hi(bias2.y)};
      f32x4 hi = {bf_lo(bias2.z), bf_hi(bias2.z), bf_lo(bias2.w), bf_hi(bias2.w)};
#pragma unroll
      for (int w = 0; w < 4; ++w) {
        lo += *(LAS const f32x4*)(pb + w * 8192);
        hi += *(LAS const f32x4*)(pb + w * 8192 + 512);
      }
      const v4u o = {cvt_pk(lo.x, lo.y), cvt_pk(lo.z, lo.w), cvt_pk(hi.x, hi.y), cvt_pk(hi.z, hi.w)};
      *(v4u*)(out + ((p2 * 4 + rh + 2 * t) * 32 + tok) * D_ + n2 * 64 + (lane & 7) * 8) = o;
      if (t == 0) STAMP(7); else STAMP(8);
    }
  }
}

extern "C" void kernel_launch(void* const* d_in, const int* in_sizes, int n_in, void* d_out, int out_size, void* d_ws, size_t ws_size,
                              hipStream_t stream) {
  mixer_mlp<<<256, 512, 0, stream>>>((const u16*)d_in[0], (const u16*)d_in[1], (const u16*)d_in[2], (const u16*)d_in[3],
                                     (const u16*)d_in[4], (u16*)d_out, (char*)d_ws);
}
```

```cpp
#define STAUX 16
#include <hip/hip_runtime.h>

typedef unsigned short u16;
typedef unsigned v4u __attribute__((ext_vector_type(4)));
typedef unsigned v2u __attribute__((ext_vector_type(2)));
typedef short bf16x8 __attribute__((ext_vector_type(8)));
typedef short s16x4 __attribute__((ext_vector_type(4)));
typedef float f32x16 __attribute__((ext_vector_type(16)));
typedef float f32x4 __attribute__((ext_vector_type(4)));
typedef float f32x2 __attribute__((ext_vector_type(2)));
#define LAS __attribute__((address_space(3)))
#define DI __device__ __forceinline__

#define DIST 2
#define ROTMODE 1

constexpr int M_ = 2048, D_ = 512, F_ = 1792;
constexpr unsigned WS_EPOCH = 0;
constexpr unsigned WS_FLAGS = 4096;
constexpr unsigned WS_STATS = 32768;
constexpr unsigned WS_H = 65536;
constexpr unsigned WS_BYTES = WS_H + 64u * 112u * 1024u;
constexpr unsigned WS_STAMP = 16u << 20;
constexpr unsigned TAG_STEP = 0x9E3779B9u;
constexpr int AUX_SC1 = 16;
constexpr unsigned SPIN_CAP = 1u << 16;
constexpr unsigned REG = 32768;

#define STAMP(i)

DI unsigned rfl(unsigned v) { return __builtin_amdgcn_readfirstlane(v); }
DI float bf_lo(unsigned d) { return __uint_as_float(d << 16); }
DI float bf_hi(unsigned d) { return __uint_as_float(d & 0xffff0000u); }
DI unsigned cvt_pk(float lo, float hi) {
  typedef __bf16 bf2 __attribute__((ext_vector_type(2)));
  f32x2 v = {lo, hi};
  return __builtin_bit_cast(unsigned, __builtin_convertvector(v, bf2));
}
DI f32x16 mfma32(v4u a, v4u b, f32x16 c) {
  return __builtin_amdgcn_mfma_f32_32x32x16_bf16(__builtin_bit_cast(bf16x8, a), __builtin_bit_cast(bf16x8, b), c, 0, 0, 0);
}
DI f32x4 mfma16(v4u a, v4u b, f32x4 c) {
  return __builtin_amdgcn_mfma_f32_16x16x32_bf16(__builtin_bit_cast(bf16x8, a), __builtin_bit_cast(bf16x8, b), c, 0, 0, 0);
}
DI v2u tr_read(LAS const char* p) {
  return __builtin_bit_cast(v2u, __builtin_amdgcn_ds_read_tr16_b64_v4i16((LAS s16x4*)p));
}
DI v4u tr_frag(LAS const char* p, int step) {
  v2u a = tr_read(p), b = tr_read(p + step);
  return v4u{a.x, a.y, b.x, b.y};
}
#define BARRIER() do { asm volatile("s_waitcnt lgkmcnt(0)\n\ts_barrier" ::: "memory"); } while (0)
#define WAITV(n) asm volatile("s_waitcnt vmcnt(%0)" :: "i"(n) : "memory")

template <int LS, int OS>
DI void dma4(__amdgpu_buffer_rsrc_t r, unsigned v, unsigned l, unsigned o) {
  asm volatile(
      "s_mov_b32 m0, %[l]\n\ts_nop 0\n\tbuffer_load_dwordx4 %[v], %[r], %[o] offen lds\n\t"
      "s_add_u32 m0, m0, %[ls]\n\ts_add_u32 %[o], %[o], %[os]\n\tbuffer_load_dwordx4 %[v], %[r], %[o] offen lds\n\t"
      "s_add_u32 m0, m0, %[ls]\n\ts_add_u32 %[o], %[o], %[os]\n\tbuffer_load_dwordx4 %[v], %[r], %[o] offen lds\n\t"
      "s_add_u32 m0, m0, %[ls]\n\ts_add_u32 %[o], %[o], %[os]\n\tbuffer_load_dwordx4 %[v], %[r], %[o] offen lds"
      : [o] "+s"(o)
      : [v] "v"(v), [r] "s"(r), [l] "s"(l), [ls] "i"(LS), [os] "i"(OS)
      : "memory", "scc");
}

__global__ __launch_bounds__(512, 2) void mixer_mlp(const u16* __restrict__ x, const u16* __restrict__ w1, const u16* __restrict__ b1,
                                                    const u16* __restrict__ w2, const u16* __restrict__ b2, u16* __restrict__ out,
                                                    char* ws) {
  __shared__ __attribute__((aligned(16))) char lds_[163840];
  LAS char* lds = (LAS char*)lds_;
  const unsigned lds0 = (unsigned)(size_t)lds;
  STAMP(0);
  const unsigned long long rt0 = __builtin_amdgcn_s_memrealtime();
  const int lane = threadIdx.x & 63;
  const int wave = rfl(threadIdx.x >> 6);
  const int wg = blockIdx.x, xcd = wg & 7, j = wg >> 3;
  const bool busy = j < 28;
  const int jd = (j * 37) >> 8;
  const int p1 = 4 * (xcd >> 1) + jd, c1 = 7 * (xcd & 1) + (j - 7 * jd);
  const int p2 = 2 * xcd + (j >> 4), rh = (j >> 3) & 1, n2 = (j + 4 * (xcd & 1)) & 7;
  const __amdgpu_buffer_rsrc_t rsW = __builtin_amdgcn_make_buffer_rsrc((void*)ws, 0, WS_BYTES, 0x00020000);
  const int h = lane >> 5, r32 = lane & 31;

  if (wave < 4) {
    const int lw = wave;
    const __amdgpu_buffer_rsrc_t rsX = __builtin_amdgcn_make_buffer_rsrc((void*)x, 0, M_ * D_ * 2, 0x00020000);
    const __amdgpu_buffer_rsrc_t rs1 = __builtin_amdgcn_make_buffer_rsrc((void*)w1, 0, D_ * F_ * 2, 0x00020000);
    const __amdgpu_buffer_rsrc_t rs2 = __builtin_amdgcn_make_buffer_rsrc((void*)w2, 0, D_ * F_ * 2, 0x00020000);
    const unsigned vx = (lane >> 3) * 1024 + (((lane & 7) ^ (4 * (lw & 1) + (lane >> 4))) * 16);
    const unsigned v1 = (lane >> 4) * (F_ * 2) + (((lane & 15) ^ ((lane >> 4) << 2)) * 16);
    const unsigned v2 = (lane >> 3) * 1024 + (((lane & 7) ^ (((lane >> 4) & 1) << 2)) * 16);
    const unsigned ox = (p1 * 128 + 8 * lw) * 1024, o1 = 4 * lw * (F_ * 2) + c1 * 256;
    int rot = 0;
    if (ROTMODE == 1) rot = (p1 + c1) & 7;
    if (ROTMODE == 2) rot = ((p1 + c1) & 1) * 4;
    if (ROTMODE == 3) rot = ((p1 + c1) & 3) * 2;
    const int rot2 = (n2 & 1) * 14;
    auto issue_stage = [&](int s) {
      const unsigned ksl = (s + rot) & 7, l = lds0 + (s & 3) * REG + lw * 1024;
      dma4<4096, 32768>(rsX, vx, l, ox + ksl * 128);
      dma4<4096, 16 * F_ * 2>(rs1, v1, l + 16384, o1 + ksl * 64 * (F_ * 2));
    };
    auto issue_group = [&](int g, int reg) {
      const unsigned l = lds0 + reg * REG + lw * 8192;
#pragma unroll
      for (int u = 0; u < 2; ++u) {
        int i = 4 * g + 2 * u + rot2;
        if (i >= 28) i -= 28;
        dma4<1024, 8192>(rs2, v2, l + u * 4096, (28 * lw + i) * 16384 + n2 * 128);
      }
    };
    if (busy) {
#pragma unroll
      for (int q = 0; q < DIST; ++q) issue_stage(q);
#pragma unroll 1
      for (int s = 0; s < 8 - DIST; ++s) {
        WAITV(8 * (DIST - 1));
        BARRIER();
        if (s == 0) STAMP(1);
        issue_stage(s + DIST);
      }
    } else {
      BARRIER();
    }
    const unsigned tag2 = rfl(*(LAS volatile unsigned*)(lds + 4 * REG + lw * 8192));
    asm volatile("s_waitcnt lgkmcnt(0)" ::: "memory");
    if (busy) {
      if (DIST == 3) { WAITV(16); BARRIER(); issue_group(0, 4); WAITV(16); BARRIER(); issue_group(1, 0); WAITV(16); STAMP(2); BARRIER(); }
      if (DIST == 2) { WAITV(8); BARRIER(); issue_group(0, 4); WAITV(8); STAMP(2); BARRIER(); issue_group(1, 0); }
    } else {
      issue_group(0, 4); issue_group(1, 0);
    }
    issue_group(2, 1);
    issue_group(3, 2);
    const int q4 = (lane & 15) >> 2, pp = lane & 3, g1 = (lane >> 4) & 1;
    LAS const char* tb0 = lds + lw * 8192 + (4 * h + q4) * 128 + (((q4 >> 1) & 1) * 64) + 32 * g1 + 8 * pp;
    LAS const char* tb1 = lds + lw * 8192 + (4 * h + q4) * 128 + ((((q4 >> 1) & 1) ^ 1) * 64) + 32 * g1 + 8 * pp;
    LAS const char *tb0r2 = tb0 + 2 * REG, *tb1r2 = tb1 + 2 * REG, *tb0r4 = tb0 + 4 * REG, *tb1r4 = tb1 + 4 * REG;
    asm volatile("" : "+v"(tb0r2), "+v"(tb1r2), "+v"(tb0r4), "+v"(tb1r4));
    v4u wr[12][2];
#pragma unroll
    for (int g = 0; g < 3; ++g) {
      const int reg = g == 0 ? 4 : g - 1;
      WAITV(24);
#pragma unroll
      for (int i = 0; i < 4; ++i) {
        wr[4 * g + i][0] = tr_frag((g == 0 ? tb0r4 : tb0 + reg * REG) + i * 2048, 1024);
        wr[4 * g + i][1] = tr_frag((g == 0 ? tb1r4 : tb1 + reg * REG) + i * 2048, 1024);
      }
#pragma unroll
      for (int i = 0; i < 4; ++i) asm volatile("" : "+v"(wr[4 * g + i][0]), "+v"(wr[4 * g + i][1]));
      issue_group(4 + g, reg);
    }
    STAMP(3);
    BARRIER();
    WAITV(0);
    STAMP(4);
    {
      const unsigned fo = WS_FLAGS + (p2 * 4 + lw) * 128 + (lane < 14 ? lane : 0) * 8;
#pragma unroll 1
      for (unsigned spins = 0; spins < SPIN_CAP; ++spins) {
        const v2u f = __builtin_amdgcn_raw_buffer_load_b64(rsW, fo, 0, AUX_SC1);
        if (__all(f.x == tag2 && f.y == ~tag2)) break;
        asm volatile("s_sleep 1" ::: "memory");
      }
      __builtin_amdgcn_fence(__ATOMIC_ACQUIRE, "wavefront");
    }
    STAMP(5);
    constexpr int RD = 7;
    const unsigned vh = lane * 16;
    auto hoff = [&](int t, int i) -> unsigned {
      int ii = i + rot2;
      if (ii >= 28) ii -= 28;
      return WS_H + (unsigned)(((p2 * 4 + rh + 2 * t) * 112 + 28 * lw + ii) * 1024);
    };
    v4u hr[RD];
#pragma unroll
    for (int i = 0; i < RD; ++i) hr[i] = __builtin_amdgcn_raw_buffer_load_b128(rsW, vh, hoff(0, i), AUX_SC1);
    __builtin_amdgcn_sched_barrier(0);
#pragma unroll 1
    for (int t = 0; t < 2; ++t) {
      f32x16 acc0 = {}, acc1 = {};
      v4u an[2][2];
      const unsigned vtail = t == 0 ? vh : 0x40000000u;
      auto rdw = [&](int i, v4u (&a)[2]) {
        const int g = i >> 2;
        LAS const char* b0 = g == 3 ? tb0r2 : (g == 4 ? tb0r4 : tb0 + (g - 5) * REG);
        LAS const char* b1 = g == 3 ? tb1r2 : (g == 4 ? tb1r4 : tb1 + (g - 5) * REG);
        a[0] = tr_frag(b0 + (i & 3) * 2048, 1024);
        a[1] = tr_frag(b1 + (i & 3) * 2048, 1024);
      };
#pragma unroll
      for (int i = 0; i < 28; ++i) {
        v4u a0, a1;
        if (i < 12) { a0 = wr[i][0]; a1 = wr[i][1]; }
        else { a0 = an[i & 1][0]; a1 = an[i & 1][1]; }
        const v4u b = hr[i % RD];
        acc0 = mfma32(a0, b, acc0);
        acc1 = mfma32(a1, b, acc1);
        __builtin_amdgcn_sched_barrier(0);
        const unsigned o = (i + RD < 28) ? hoff(t, i + RD) : hoff(1, i + RD - 28);
        hr[i % RD] = __builtin_amdgcn_raw_buffer_load_b128(rsW, (i + RD < 28) ? vh : vtail, o, AUX_SC1);
        if (i + 2 >= 12 && i + 2 < 28) rdw(i + 2, an[i & 1]);
        __builtin_amdgcn_sched_barrier(0);
      }
      LAS char* pb = lds + (3 + t) * REG + lw * 8192;
#pragma unroll
      for (int g = 0; g < 4; ++g) {
        *(LAS f32x4*)(pb + g * 1024 + ((lane ^ (g << 2)) * 16)) = f32x4{acc0[4 * g], acc0[4 * g + 1], acc0[4 * g + 2], acc0[4 * g + 3]};
        *(LAS f32x4*)(pb + 4096 + g * 1024 + ((lane ^ (g << 2)) * 16)) = f32x4{acc1[4 * g], acc1[4 * g + 1], acc1[4 * g + 2], acc1[4 * g + 3]};
      }
      if (t == 0) STAMP(6); else STAMP(7);
      BARRIER();
    }
  } else {
    const int cw = wave - 4;
    const unsigned e1 = __builtin_amdgcn_raw_buffer_load_b32(rsW, 0, WS_EPOCH + p1 * 256, AUX_SC1);
    const unsigned e2 = __builtin_amdgcn_raw_buffer_load_b32(rsW, 0, WS_EPOCH + p2 * 256, AUX_SC1);
    const int srow = p2 * 128 + 8 * (j & 15) + 2 * cw + h;
    const v4u* xr = (const v4u*)(x + srow * D_);
    const v4u xa = xr[r32], xb = xr[32 + r32];
    const unsigned bias1 = busy ? b1[c1 * 128 + cw * 32 + r32] : 0;
    const v4u bias2 = *(const v4u*)(b2 + n2 * 64 + (lane & 7) * 8);
    const unsigned tag1 = rfl(e1) + TAG_STEP, tag2 = rfl(e2) + TAG_STEP;
    if (lane == 0) *(LAS volatile unsigned*)(lds + 4 * REG + cw * 8192) = tag2;
    {
      float v[16];
#pragma unroll
      for (int i = 0; i < 4; ++i) { v[2 * i] = bf_lo(xa[i]); v[2 * i + 1] = bf_hi(xa[i]); v[8 + 2 * i] = bf_lo(xb[i]); v[9 + 2 * i] = bf_hi(xb[i]); }
      float s = 0.f;
#pragma unroll
      for (int i = 0; i < 16; ++i) s += v[i];
#pragma unroll
      for (int o = 16; o; o >>= 1) s += __shfl_xor(s, o);
      const float mean = s * (1.f / D_);
      float q = 0.f;
#pragma unroll
      for (int i = 0; i < 16; ++i) { const float d = v[i] - mean; q = fmaf(d, d, q); }
#pragma unroll
      for (int o = 16; o; o >>= 1) q += __shfl_xor(q, o);
      const float sig = __builtin_sqrtf(q * (1.f / D_) + 1e-5f);
      const unsigned m1 = cvt_pk(-mean, sig);
      const unsigned m2 = cvt_pk(-mean - bf_lo(m1), sig - bf_hi(m1));
      const v4u gr = {(m1 & 0xffffu) | (m2 << 16), tag2, (m1 >> 16) | (m2 & 0xffff0000u), tag2};
      if (r32 == 0) __builtin_amdgcn_raw_buffer_store_b128(gr, rsW, WS_STATS + srow * 16, 0, AUX_SC1);
    }
    STAMP(1);
    if (busy) {
      const int q4 = (lane & 15) >> 2, pp = lane & 3, g1 = (lane >> 4) & 1;
      const int aoff = 16384 + (8 * h + q4) * 256 + ((cw ^ q4) * 64) + 32 * g1 + 8 * pp;
      int boff[4];
#pragma unroll
      for (int ks = 0; ks < 4; ++ks) boff[ks] = r32 * 128 + (((2 * ks + h) ^ ((r32 >> 1) & 7)) * 16);
      const unsigned one2 = 0x3f803f80u;
      const unsigned selv = ((((lane & 15) >> 2) ^ (lane >> 4)) & 1) ? 0u : one2;
      const v4u sel = {selv, selv, selv, selv};
      f32x16 acc[4] = {};
      f32x4 cs = {};
      v4u A, B[4], st[4];
      BARRIER();
      STAMP(2);
      auto rd = [&](int s, int ks, v4u& a, v4u (&b)[4]) {
        LAS const char* base = lds + (s & 3) * REG;
        a = tr_frag(base + aoff + ks * 4096, 1024);
#pragma unroll
        for (int t = 0; t < 4; ++t) b[t] = *(LAS const v4u*)(base + boff[ks] + t * 4096);
      };
      rd(0, 0, A, B);
#pragma unroll
      for (int s = 0; s < 8; ++s) {
        if (s == 5) {
#pragma unroll
          for (int t = 0; t < 4; ++t) st[t] = __builtin_amdgcn_raw_buffer_load_b128(rsW, WS_STATS + (p1 * 128 + t * 32 + r32) * 16, 0, AUX_SC1);
        }
#pragma unroll
        for (int ks = 0; ks < 4; ++ks) {
          v4u An, Bn[4];
          if (ks < 3) rd(s, ks + 1, An, Bn);
          else if (s < 7) { BARRIER(); rd(s + 1, 0, An, Bn); }
          cs = mfma16(sel, A, cs);
#pragma unroll
          for (int t = 0; t < 4; ++t) acc[t] = mfma32(A, B[t], acc[t]);
          A = An;
#pragma unroll
          for (int t = 0; t < 4; ++t) B[t] = Bn[t];
          __builtin_amdgcn_sched_barrier(0);
        }
      }
      STAMP(3);
#pragma unroll 1
      for (unsigned spins = 0; spins < SPIN_CAP; ++spins) {
        bool ok = true;
#pragma unroll
        for (int t = 0; t < 4; ++t) ok &= st[t].y == tag1 && st[t].w == tag1;
        if (__all(ok)) break;
        asm volatile("s_sleep 1" ::: "memory");
#pragma unroll
        for (int t = 0; t < 4; ++t) st[t] = __builtin_amdgcn_raw_buffer_load_b128(rsW, WS_STATS + (p1 * 128 + t * 32 + r32) * 16, 0, AUX_SC1);
      }
      {
        const float s0 = cs[0];
        const unsigned a1 = cvt_pk(s0, s0);
        const float r1 = s0 - bf_lo(a1);
        const unsigned a2 = cvt_pk(r1, r1);
        const float r2 = r1 - bf_lo(a2);
        const unsigned a3 = cvt_pk(r2, r2);
        const v4u ax = {h ? (bias1 | (bias1 << 16)) : a1, h ? 0u : a2, h ? 0u : a3, 0u};
#pragma unroll
        for (int t = 0; t < 4; ++t) {
          const unsigned bv = h ? st[t].z : st[t].x;
          acc[t] = mfma32(ax, v4u{bv, bv, bv, bv}, acc[t]);
        }
      }
      STAMP(4);
      constexpr float C0 = -2.3022081985f, C1 = -0.1029432397f;
#pragma unroll
      for (int t = 0; t < 4; ++t) {
        const float sig = bf_lo(st[t].z) + bf_hi(st[t].z);
        const float r = __builtin_amdgcn_rcpf(sig);
        const float k0 = C0 * r, k1 = C1 * r * r * r;
        unsigned o[8];
#pragma unroll
        for (int i = 0; i < 8; ++i) {
          const f32x2 v = {acc[t][2 * i], acc[t][2 * i + 1]};
          const f32x2 u = v * (v * v * k1 + k0);
          const f32x2 e = {__builtin_amdgcn_exp2f(u.x), __builtin_amdgcn_exp2f(u.y)};
          const f32x2 d = e * sig + sig;
          const f32x2 qq = {__builtin_amdgcn_rcpf(d.x), __builtin_amdgcn_rcpf(d.y)};
          const f32x2 g = v * qq;
          o[i] = cvt_pk(g.x, g.y);
        }
        const unsigned ho = WS_H + ((p1 * 4 + t) * 112 + 8 * c1 + 2 * cw) * 1024;
        __builtin_amdgcn_raw_buffer_store_b128(v4u{o[0], o[1], o[2], o[3]}, rsW, lane * 16 + ho, 0, AUX_SC1);
        __builtin_amdgcn_raw_buffer_store_b128(v4u{o[4], o[5], o[6], o[7]}, rsW, lane * 16 + ho + 1024, 0, AUX_SC1);
      }
      STAMP(5);
      WAITV(0);
      STAMP(6);
      {
        const int idx = 4 * c1 + cw, kq = (idx * 74) >> 10;
        if (lane == 0) __builtin_amdgcn_raw_buffer_store_b64(v2u{tag1, ~tag1}, rsW, WS_FLAGS + (p1 * 4 + kq) * 128 + (idx - 14 * kq) * 8, 0, AUX_SC1);
      }
    } else {
      BARRIER();
    }
    BARRIER();
    const int nb = (lane & 7) >> 2, g = lane & 3, tok = (lane >> 3) + 8 * cw;
#pragma unroll 1
    for (int t = 0; t < 2; ++t) {
      BARRIER();
      if (t == 0 && (j & 15) == 0 && cw == 0 && lane == 0) __builtin_amdgcn_raw_buffer_store_b32(tag2, rsW, WS_EPOCH + p2 * 256, 0, AUX_SC1);
      LAS const char* pb = lds + (3 + t) * REG + nb * 4096 + g * 1024 + ((tok ^ (g << 2)) * 16);
      f32x4 lo = {bf_lo(bias2.x), bf_hi(bias2.x), bf_lo(bias2.y), bf_hi(bias2.y)};
      f32x4 hi = {bf_lo(bias2.z), bf_hi(bias2.z), bf_lo(bias2.w), bf_hi(bias2.w)};
#pragma unroll
      for (int w = 0; w < 4; ++w) {
        lo += *(LAS const f32x4*)(pb + w * 8192);
        hi += *(LAS const f32x4*)(pb + w * 8192 + 512);
      }
      const v4u o = {cvt_pk(lo.x, lo.y), cvt_pk(lo.z, lo.w), cvt_pk(hi.x, hi.y), cvt_pk(hi.z, hi.w)};
      __builtin_amdgcn_raw_buffer_store_b128(o, __builtin_amdgcn_make_buffer_rsrc((void*)out, 0, M_ * D_ * 2, 0x00020000), (((p2 * 4 + rh + 2 * t) * 32 + tok) * D_ + n2 * 64 + (lane & 7) * 8) * 2, 0, STAUX);
      if (t == 0) STAMP(7); else STAMP(8);
    }
  }
  while (__builtin_amdgcn_s_memrealtime() - rt0 < 4000ull) __builtin_amdgcn_s_sleep(2);
}

extern "C" void kernel_launch(void* const* d_in, const int* in_sizes, int n_in, void* d_out, int out_size, void* d_ws, size_t ws_size,
                              hipStream_t stream) {
  mixer_mlp<<<256, 512, 0, stream>>>((const u16*)d_in[0], (const u16*)d_in[1], (const u16*)d_in[2], (const u16*)d_in[3],
                                     (const u16*)d_in[4], (u16*)d_out, (char*)d_ws);
}
```

```cpp
#include <hip/hip_runtime.h>

__global__ __launch_bounds__(512, 2) __attribute__((aligned(4096))) void mixer_mlp(const void* x, const void* w1, const void* b1,
                                                                                   const void* w2, const void* b2, void* out, void* ws) {
  __shared__ __attribute__((aligned(16))) char lds[163840];
  asm volatile(
      ".Lstart_k:\n"
      "s_getpc_b64 s[62:63]\n"
      "s_load_dwordx2 s[8:9], %0, 0\n"
      "s_load_dwordx2 s[12:13], %0, 8\n"
      "s_load_dwordx2 s[24:25], %0, 16\n"
      "s_load_dwordx2 s[16:17], %0, 24\n"
      "s_load_dwordx2 s[28:29], %0, 32\n"
      "s_load_dwordx2 s[32:33], %0, 40\n"
      "s_load_dwordx2 s[20:21], %0, 48\n"
      "v_readfirstlane_b32 s50, %2\n"
      "v_and_b32 v1, 63, %2\n"
      "s_and_b32 s37, %1, 7\n"
      "s_lshr_b32 s38, %1, 3\n"
      "s_bfe_u32 s36, s50, 0x20006\n"
      "s_mul_i32 s39, s38, 37\n"
      "s_lshr_b32 s39, s39, 8\n"
      "s_mul_i32 s40, s39, 7\n"
      "s_sub_u32 s40, s38, s40\n"
      "s_lshr_b32 s41, s37, 1\n"
      "s_lshl2_add_u32 s41, s41, s39\n"
      "s_and_b32 s42, s37, 1\n"
      "s_mul_i32 s42, s42, 7\n"
      "s_add_u32 s42, s42, s40\n"
      "s_cmp_lt_u32 s38, 28\n"
      "s_cselect_b32 s48, 1, 0\n"
      "s_cselect_b32 s43, s39, s40\n"
      "s_cselect_b32 s44, s40, 7\n"
      "s_lshr_b32 s45, s43, 1\n"
      "s_lshl1_add_u32 s45, s37, s45\n"
      "s_and_b32 s46, s43, 1\n"
      "s_mov_b32 s10, 2097152\n"
      "s_mov_b32 s11, 0x20000\n"
      "s_mov_b32 s22, 7602176\n"
      "s_mov_b32 s23, s11\n"
      "s_lshl_b32 s88, s45, 8\n"
      "s_lshl2_add_u32 s87, s45, s36\n"
      "s_mul_i32 s87, s87, 1536\n"
      "s_add_u32 s87, s87, 65536\n"
      "s_bitcmp1_b32 s50, 8\n"
      "s_cbranch_scc1 .Ll_role\n"
      "s_setprio 3\n"
      "s_lshl1_add_u32 s53, s45, s46\n"
      "s_lshl3_add_u32 s53, s53, s44\n"
      "s_and_b32 s54, s53, 15\n"
      "s_lshl2_add_u32 s53, s53, s36\n"
      "s_lshl_b32 s53, s53, 11\n"
      "v_and_b32 v16, 32, v1\n"
      "v_add_u32 v16, v16, v1\n"
      "v_lshlrev_b32 v16, 4, v16\n"
      "s_mov_b32 s26, 3584\n"
      "s_mov_b32 s27, s11\n"
      "s_movk_i32 s30, 1024\n"
      "s_mov_b32 s31, s11\n"
      "v_mov_b32 v11, 0\n"
      "v_lshlrev_b32 v17, 8, v1\n"
      "s_lshl2_add_u32 s52, s42, s36\n"
      "s_lshl_b32 s52, s52, 6\n"
      "v_and_b32 v7, 15, v1\n"
      "v_lshlrev_b32 v18, 1, v7\n"
      "v_and_b32 v19, 7, v1\n"
      "v_lshlrev_b32 v19, 4, v19\n"
      "s_lshl_b32 s59, s41, 8\n"
      "s_cmp_lg_u32 s54, 0\n"
      "s_waitcnt lgkmcnt(0)\n"
      "buffer_load_dwordx4 v[48:51], v16, s[8:11], s53 offen\n"
      "buffer_load_dwordx4 v[52:55], v16, s[8:11], s53 offen offset:512\n"
      "buffer_load_dword v32, off, s[20:23], s59 sc1\n"
      "buffer_load_dword v33, off, s[20:23], s88 sc1\n"
      "buffer_load_ushort v8, v18, s[24:27], s52 offen\n"
      "buffer_load_ushort v9, v18, s[24:27], s52 offen offset:32\n"
      "s_lshr_b32 s47, s16, 7\n"
      "s_sub_u32 s47, s44, s47\n"
      "s_add_u32 s47, s47, 4\n"
      "s_and_b32 s47, s47, 7\n"
      "s_lshl_b32 s56, s47, 7\n"
      "buffer_load_dwordx4 v[112:115], v19, s[28:31], s56 offen\n"
      "s_setprio 0\n"
      "s_branch .Lc_role\n"
      ".Ll_role:\n"
      "s_mov_b32 s18, 1835008\n"
      "s_mov_b32 s19, s11\n"
      "s_mov_b64 s[14:15], s[18:19]\n"
      "v_lshrrev_b32 v234, 3, v1\n"
      "v_lshrrev_b32 v235, 4, v1\n"
      "v_and_b32 v236, 7, v1\n"
      "v_xor_b32 v237, v236, v235\n"
      "v_lshlrev_b32 v237, 4, v237\n"
      "v_lshl_add_u32 v224, v234, 10, v237\n"
      "v_xor_b32 v225, 64, v224\n"
      "v_add_u32 v225, 7168, v225\n"
      "v_and_b32 v237, 15, v1\n"
      "v_lshlrev_b32 v226, 2, v235\n"
      "v_xor_b32 v237, v237, v226\n"
      "v_lshlrev_b32 v237, 4, v237\n"
      "v_mul_u32_u24 v226, 3584, v235\n"
      "v_add_u32 v226, v226, v237\n"
      "v_add_u32 v227, 13312, v226\n"
      "v_xor_b32 v228, 32, v226\n"
      "v_add_u32 v228, 26624, v228\n"
      "v_add_u32 v229, 13312, v228\n"
      "s_cmp_lt_u32 s37, 4\n"
      "s_cselect_b32 s81, 1, 1\n"
      "s_and_b32 s81, s81, s41\n"
      "s_and_b32 s50, s37, 2\n"
      "s_lshl1_add_u32 s81, s50, s81\n"
      "s_lshl3_add_u32 s79, s41, s36\n"
      "s_lshl_b32 s79, s79, 14\n"
      "s_mul_i32 s80, s36, 57344\n"
      "s_lshl_b32 s50, s42, 8\n"
      "s_add_u32 s80, s80, s50\n"
      "s_lshl_b32 s78, s36, 12\n"
      "s_add_u32 s86, s78, 98304\n"
      "s_lshl_b32 s77, s36, 11\n"
      "s_sub_u32 s77, 16384, s77\n"
      "s_add_u32 s66, s62, .Ll_stage-.Lstart_k-4\n"
      "s_addc_u32 s67, s63, 0\n"
      "s_add_u32 s64, s62, .Ll_s1-.Lstart_k-4\n"
      "s_addc_u32 s65, s63, 0\n"
      "s_cmp_eq_u32 s48, 0\n"
      "s_waitcnt lgkmcnt(0)\n"
      "buffer_load_dword v7, off, s[20:23], s88 sc1\n"
      "s_cbranch_scc1 .Ll_idle0\n"
      ".Ll_stage:\n"
      "s_and_b32 s81, s81, 7\n"
      "s_mul_i32 s51, s81, 229376\n"
      "s_mov_b32 m0, s78\n"
      "s_add_u32 s51, s51, s80\n"
      "buffer_load_dwordx4 v226, s[12:15], s51 offen lds\n"
      "buffer_load_dwordx4 v227, s[12:15], s51 offen offset:1024 lds\n"
      "s_lshl_b32 s50, s81, 7\n"
      "s_add_u32 m0, s78, s77\n"
      "s_add_u32 s50, s50, s79\n"
      "buffer_load_dwordx4 v224, s[8:11], s50 offen lds\n"
      "buffer_load_dwordx4 v225, s[8:11], s50 offen offset:1024 lds\n"
      "s_mov_b32 m0, s78\n"
      "s_add_u32 s81, s81, 1\n"
      "buffer_load_dwordx4 v228, s[12:15], s51 offen offset:2048 lds\n"
      "buffer_load_dwordx4 v229, s[12:15], s51 offen offset:3072 lds\n"
      "s_add_u32 s78, s78, 24576\n"
      "s_cmp_eq_u32 s78, s86\n"
      "s_cselect_b32 s50, 98304, 0\n"
      "s_sub_u32 s78, s78, s50\n"
      "s_setpc_b64 s[64:65]\n"
      ".Lc_role:\n"
      "s_cmp_eq_u32 s48, 1\n"
      "s_cbranch_scc1 .Lc_noipf\n"
      "s_lshl_b32 s57, s36, 12\n"
      "v_and_b32 v16, 31, v1\n"
      "v_lshl_add_u32 v16, v16, 7, s57\n"
      "v_min_u32 v16, .Lend_k-.Lstart_k-4, v16\n"
      "global_load_dword v253, v16, s[62:63]\n"
      ".Lc_noipf:\n"
      "v_lshlrev_b32 v6, 4, v1\n"
      "v_lshrrev_b32 v20, 4, v1\n"
      "v_bfe_u32 v21, v1, 2, 2\n"
      "v_lshl_add_u32 v2, v20, 3, v21\n"
      "v_lshlrev_b32 v2, 8, v2\n"
      "v_xor_b32 v21, s36, v21\n"
      "v_lshl_add_u32 v2, v21, 6, v2\n"
      "v_and_b32 v21, 16, v1\n"
      "v_lshl_add_u32 v2, v21, 1, v2\n"
      "v_and_b32 v21, 3, v1\n"
      "v_lshl_add_u32 v2, v21, 3, v2\n"
      "v_xor_b32 v3, 32, v2\n"
      "v_bfe_u32 v21, v1, 1, 3\n"
      "v_xor_b32 v21, v21, v20\n"
      "v_lshlrev_b32 v21, 4, v21\n"
      "v_lshl_add_u32 v4, v7, 7, v21\n"
      "v_add_u32 v4, 16384, v4\n"
      "v_xor_b32 v5, 64, v4\n"
      "v_add_u32 v116, 49152, v2\n"
      "v_add_u32 v117, 49152, v3\n"
      "v_add_u32 v118, 49152, v4\n"
      "v_add_u32 v119, 49152, v5\n"
      "s_lshl_b32 s50, s41, 11\n"
      "s_add_u32 s50, s50, 32768\n"
      "v_lshl_add_u32 v7, v1, 4, s50\n"
      "v_mov_b32 v12, 0x3f803f80\n"
      "v_mov_b32 v13, 0x3f803f80\n"
      "v_mov_b32 v14, 0x3f803f80\n"
      "v_mov_b32 v15, 0x3f803f80\n"
      "v_cmp_eq_u32 s[70:71], 0, v20\n"
      "v_cmp_eq_u32 s[72:73], 1, v20\n"
      "v_cmp_eq_u32 s[74:75], 2, v20\n"
      "s_mov_b32 s58, 0xffff0000\n"
      "s_lshr_b32 s53, s53, 6\n"
      "s_add_u32 s53, s53, 32768\n"
      "s_mul_i32 s69, s41, 452\n"
      "s_mul_i32 s50, s36, 28\n"
      "s_add_u32 s69, s69, s50\n"
      "s_lshl1_add_u32 s69, s42, s69\n"
      "s_lshl_b32 s69, s69, 10\n"
      "s_add_u32 s69, s69, 196608\n"
      "s_lshl2_add_u32 s84, s41, s36\n"
      "s_mul_i32 s84, s84, 1536\n"
      "s_add_u32 s84, s84, 65536\n"
      "s_lshl3_add_u32 s85, s42, s84\n"
      "s_lshl_b32 s83, 1, s42\n"
      "s_add_u32 s66, s62, .Lc_g0-.Lstart_k-4\n"
      "s_addc_u32 s67, s63, 0\n"
      "s_add_u32 s64, s62, .Lc_b-.Lstart_k-4\n"
      "s_addc_u32 s65, s63, 0\n"
      "s_mov_b32 s50, 0x05040100\n"
      "s_mov_b32 s51, 0x07060302\n"
      "s_branch .Lc_stats\n"
      ".Ll_idle0:\n"
      "s_memtime s[4:5]\n"
      "s_lshl2_add_u32 s89, s43, s36\n"
      "s_lshl_b32 s89, s89, 13\n"
      "v_lshlrev_b32 v242, 7, v1\n"
      "s_lshl_b32 s54, s37, 17\n"
      "s_add_u32 s54, s54, s89\n"
      "s_waitcnt vmcnt(0) lgkmcnt(0)\n"
      "s_barrier\n"
      "buffer_load_dword v240, v242, s[16:19], s54 offen\n"
      "s_cmp_ge_u32 s37, 6\n"
      "s_cbranch_scc1 .Ll_pf_x\n"
      "s_add_u32 s54, s54, 1048576\n"
      "buffer_load_dword v240, v242, s[16:19], s54 offen\n"
      ".Ll_pf_x:\n"
      "s_add_u32 s54, s89, 1572864\n"
      ".Ll_pfa_loop:\n"
      "s_waitcnt vmcnt(1)\n"
      "buffer_load_dword v240, v242, s[16:19], s54 offen\n"
      "s_add_u32 s54, s54, 131072\n"
      "s_cmp_lt_u32 s54, 1835008\n"
      "s_cbranch_scc1 .Ll_pfa_loop\n"
      "s_branch .Ll_cont\n"
      ".Ll_s1:\n"
      "s_swappc_b64 s[64:65], s[66:67]\n"
      ".Ll_cont:\n"
      "s_lshr_b32 s47, s16, 7\n"
      "s_sub_u32 s47, s44, s47\n"
      "s_add_u32 s47, s47, 4\n"
      "s_and_b32 s47, s47, 7\n"
      "v_lshlrev_b32 v237, 1, v235\n"
      "v_xor_b32 v237, v237, v236\n"
      "v_lshlrev_b32 v237, 4, v237\n"
      "v_lshl_add_u32 v230, v234, 10, v237\n"
      "v_add_u32 v231, 7168, v230\n"
      "v_add_u32 v232, 7168, v231\n"
      "v_add_u32 v233, 7168, v232\n"
      "s_cmp_eq_u32 s48, 1\n"
      "s_lshl_b32 s83, s43, 1\n"
      "s_cmp_eq_u32 s48, 1\n"
      "s_cselect_b32 s83, 0, s83\n"
      "s_lshl_b32 s85, s36, 15\n"
      "s_lshl_b32 s50, s47, 7\n"
      "s_add_u32 s85, s85, s50\n"
      "s_lshl_b32 s84, s36, 13\n"
      "s_lshl2_add_u32 s50, s45, s46\n"
      "s_mul_i32 s50, s50, 115712\n"
      "s_mul_i32 s69, s36, 28672\n"
      "s_add_u32 s69, s69, s50\n"
      "s_add_u32 s69, s69, 196608\n"
      "s_lshl_b32 s50, s83, 12\n"
      "s_add_u32 s70, s50, s69\n"
      "s_lshl_b32 s50, s83, 18\n"
      "s_add_u32 s24, s50, s85\n"
      "s_add_u32 s83, s83, 1\n"
      "s_cmp_eq_u32 s83, 7\n"
      "s_cselect_b32 s83, 0, s83\n"
      "s_lshl_b32 s50, s83, 12\n"
      "s_add_u32 s71, s50, s69\n"
      "s_lshl_b32 s50, s83, 18\n"
      "s_add_u32 s25, s50, s85\n"
      "s_add_u32 s83, s83, 1\n"
      "s_cmp_eq_u32 s83, 7\n"
      "s_cselect_b32 s83, 0, s83\n"
      "s_lshl_b32 s50, s83, 12\n"
      "s_add_u32 s72, s50, s69\n"
      "s_lshl_b32 s50, s83, 18\n"
      "s_add_u32 s26, s50, s85\n"
      "s_add_u32 s83, s83, 1\n"
      "s_cmp_eq_u32 s83, 7\n"
      "s_cselect_b32 s83, 0, s83\n"
      "s_lshl_b32 s50, s83, 12\n"
      "s_add_u32 s73, s50, s69\n"
      "s_lshl_b32 s50, s83, 18\n"
      "s_add_u32 s27, s50, s85\n"
      "s_add_u32 s83, s83, 1\n"
      "s_cmp_eq_u32 s83, 7\n"
      "s_cselect_b32 s83, 0, s83\n"
      "s_lshl_b32 s50, s83, 12\n"
      "s_add_u32 s74, s50, s69\n"
      "s_lshl_b32 s50, s83, 18\n"
      "s_add_u32 s28, s50, s85\n"
      "s_add_u32 s83, s83, 1\n"
      "s_cmp_eq_u32 s83, 7\n"
      "s_cselect_b32 s83, 0, s83\n"
      "s_lshl_b32 s50, s83, 12\n"
      "s_add_u32 s75, s50, s69\n"
      "s_lshl_b32 s50, s83, 18\n"
      "s_add_u32 s29, s50, s85\n"
      "s_add_u32 s83, s83, 1\n"
      "s_cmp_eq_u32 s83, 7\n"
      "s_cselect_b32 s83, 0, s83\n"
      "s_lshl_b32 s50, s83, 12\n"
      "s_add_u32 s76, s50, s69\n"
      "s_lshl_b32 s50, s83, 18\n"
      "s_add_u32 s30, s50, s85\n"
      "v_lshrrev_b32 v234, 2, v1\n"
      "v_lshl_add_u32 v2, v234, 7, s84\n"
      "v_bfe_u32 v235, v1, 3, 2\n"
      "v_lshl_add_u32 v2, v235, 5, v2\n"
      "v_and_b32 v235, 3, v1\n"
      "v_lshl_add_u32 v2, v235, 3, v2\n"
      "v_xor_b32 v3, 32, v2\n"
      "v_xor_b32 v4, 64, v2\n"
      "v_xor_b32 v5, 96, v2\n"
      "v_lshlrev_b32 v6, 4, v1\n"
      "v_and_b32 v235, 32, v1\n"
      "v_lshlrev_b32 v235, 1, v235\n"
      "v_xor_b32 v248, v235, v6\n"
      "s_add_u32 s52, s84, 131072\n"
      "v_add_u32 v248, s52, v248\n"
      "s_add_u32 s87, s87, 512\n"
      "s_cmp_eq_u32 s48, 0\n"
      "s_cbranch_scc1 .Ll_w2\n"
      "s_swappc_b64 s[64:65], s[66:67]\n"
      "s_and_b32 s81, s81, 7\n"
      "s_mul_i32 s51, s81, 229376\n"
      "s_add_u32 s51, s51, s80\n"
      "s_lshl_b32 s50, s81, 7\n"
      "s_add_u32 s50, s50, s79\n"
      "s_add_u32 s81, s81, 1\n"
      "s_sub_u32 m0, s86, 24576\n"
      "s_waitcnt vmcnt(12) lgkmcnt(0)\n"
      "s_barrier\n"
      "s_sleep 1\n"
      "buffer_load_dwordx4 v226, s[12:15], s51 offen offset:0 lds\n"
      "buffer_load_dwordx4 v227, s[12:15], s51 offen offset:1024 lds\n"
      "buffer_load_dwordx4 v228, s[12:15], s51 offen offset:2048 lds\n"
      "buffer_load_dwordx4 v229, s[12:15], s51 offen offset:3072 lds\n"
      "s_add_u32 m0, m0, s77\n"
      "s_nop 0\n"
      "buffer_load_dwordx4 v224, s[8:11], s50 offen offset:0 lds\n"
      "buffer_load_dwordx4 v225, s[8:11], s50 offen offset:1024 lds\n"
      "s_and_b32 s81, s81, 7\n"
      "s_mul_i32 s51, s81, 229376\n"
      "s_add_u32 s51, s51, s80\n"
      "s_lshl_b32 s50, s81, 7\n"
      "s_add_u32 s50, s50, s79\n"
      "s_add_u32 s81, s81, 1\n"
      "s_sub_u32 m0, s86, 98304\n"
      "s_waitcnt vmcnt(12)\n"
      "s_barrier\n"
      "buffer_load_dwordx4 v226, s[12:15], s51 offen offset:0 lds\n"
      "buffer_load_dwordx4 v227, s[12:15], s51 offen offset:1024 lds\n"
      "buffer_load_dwordx4 v228, s[12:15], s51 offen offset:2048 lds\n"
      "buffer_load_dwordx4 v229, s[12:15], s51 offen offset:3072 lds\n"
      "s_add_u32 m0, m0, s77\n"
      "s_nop 0\n"
      "buffer_load_dwordx4 v224, s[8:11], s50 offen offset:0 lds\n"
      "buffer_load_dwordx4 v225, s[8:11], s50 offen offset:1024 lds\n"
      "s_and_b32 s81, s81, 7\n"
      "s_mul_i32 s51, s81, 229376\n"
      "s_add_u32 s51, s51, s80\n"
      "s_lshl_b32 s50, s81, 7\n"
      "s_add_u32 s50, s50, s79\n"
      "s_add_u32 s81, s81, 1\n"
      "s_sub_u32 m0, s86, 73728\n"
      "s_waitcnt vmcnt(12)\n"
      "s_barrier\n"
      "buffer_load_dwordx4 v226, s[12:15], s51 offen offset:0 lds\n"
      "buffer_load_dwordx4 v227, s[12:15], s51 offen offset:1024 lds\n"
      "buffer_load_dwordx4 v228, s[12:15], s51 offen offset:2048 lds\n"
      "buffer_load_dwordx4 v229, s[12:15], s51 offen offset:3072 lds\n"
      "s_add_u32 m0, m0, s77\n"
      "s_nop 0\n"
      "buffer_load_dwordx4 v224, s[8:11], s50 offen offset:0 lds\n"
      "buffer_load_dwordx4 v225, s[8:11], s50 offen offset:1024 lds\n"
      "s_and_b32 s81, s81, 7\n"
      "s_mul_i32 s51, s81, 229376\n"
      "s_add_u32 s51, s51, s80\n"
      "s_lshl_b32 s50, s81, 7\n"
      "s_add_u32 s50, s50, s79\n"
      "s_add_u32 s81, s81, 1\n"
      "s_sub_u32 m0, s86, 49152\n"
      "s_waitcnt vmcnt(12)\n"
      "s_barrier\n"
      "buffer_load_dwordx4 v226, s[12:15], s51 offen offset:0 lds\n"
      "buffer_load_dwordx4 v227, s[12:15], s51 offen offset:1024 lds\n"
      "buffer_load_dwordx4 v228, s[12:15], s51 offen offset:2048 lds\n"
      "buffer_load_dwordx4 v229, s[12:15], s51 offen offset:3072 lds\n"
      "s_add_u32 m0, m0, s77\n"
      "s_nop 0\n"
      "buffer_load_dwordx4 v224, s[8:11], s50 offen offset:0 lds\n"
      "buffer_load_dwordx4 v225, s[8:11], s50 offen offset:1024 lds\n"
      "s_and_b32 s81, s81, 7\n"
      "s_mul_i32 s51, s81, 229376\n"
      "s_add_u32 s51, s51, s80\n"
      "s_lshl_b32 s50, s81, 7\n"
      "s_add_u32 s50, s50, s79\n"
      "s_add_u32 s81, s81, 1\n"
      "s_sub_u32 m0, s86, 24576\n"
      "s_waitcnt vmcnt(12)\n"
      "s_barrier\n"
      "buffer_load_dwordx4 v226, s[12:15], s51 offen offset:0 lds\n"
      "buffer_load_dwordx4 v227, s[12:15], s51 offen offset:1024 lds\n"
      "buffer_load_dwordx4 v228, s[12:15], s51 offen offset:2048 lds\n"
      "buffer_load_dwordx4 v229, s[12:15], s51 offen offset:3072 lds\n"
      "s_add_u32 m0, m0, s77\n"
      "s_nop 0\n"
      "buffer_load_dwordx4 v224, s[8:11], s50 offen offset:0 lds\n"
      "buffer_load_dwordx4 v225, s[8:11], s50 offen offset:1024 lds\n"
      "s_waitcnt vmcnt(12)\n"
      "s_barrier\n"
      "s_add_u32 s79, s79, 65536\n"
      "s_lshl_b32 s78, s36, 11\n"
      "s_add_u32 m0, s78, 90112\n"
      "s_and_b32 s81, s81, 7\n"
      "s_lshl_b32 s50, s81, 7\n"
      "s_add_u32 m0, m0, 8192\n"
      "s_add_u32 s50, s50, s79\n"
      "buffer_load_dwordx4 v224, s[8:11], s50 offen lds\n"
      "buffer_load_dwordx4 v225, s[8:11], s50 offen offset:1024 lds\n"
      "s_add_u32 s81, s81, 1\n"
      "s_and_b32 s81, s81, 7\n"
      "s_lshl_b32 s50, s81, 7\n"
      "s_add_u32 m0, m0, 8192\n"
      "s_add_u32 s50, s50, s79\n"
      "buffer_load_dwordx4 v224, s[8:11], s50 offen lds\n"
      "buffer_load_dwordx4 v225, s[8:11], s50 offen offset:1024 lds\n"
      "s_add_u32 s81, s81, 1\n"
      "s_and_b32 s81, s81, 7\n"
      "s_lshl_b32 s50, s81, 7\n"
      "s_add_u32 m0, m0, 8192\n"
      "s_add_u32 s50, s50, s79\n"
      "buffer_load_dwordx4 v224, s[8:11], s50 offen lds\n"
      "buffer_load_dwordx4 v225, s[8:11], s50 offen offset:1024 lds\n"
      "s_add_u32 s81, s81, 1\n"
      "s_waitcnt vmcnt(12)\n"
      "s_barrier\n"
      "s_and_b32 s81, s81, 7\n"
      "s_lshl_b32 s50, s81, 7\n"
      "s_add_u32 m0, m0, 8192\n"
      "s_add_u32 s50, s50, s79\n"
      "buffer_load_dwordx4 v224, s[8:11], s50 offen lds\n"
      "buffer_load_dwordx4 v225, s[8:11], s50 offen offset:1024 lds\n"
      "s_add_u32 s81, s81, 1\n"
      "s_and_b32 s81, s81, 7\n"
      "s_lshl_b32 s50, s81, 7\n"
      "s_add_u32 m0, m0, 8192\n"
      "s_add_u32 s50, s50, s79\n"
      "buffer_load_dwordx4 v224, s[8:11], s50 offen lds\n"
      "buffer_load_dwordx4 v225, s[8:11], s50 offen offset:1024 lds\n"
      "s_add_u32 s81, s81, 1\n"
      "s_and_b32 s81, s81, 7\n"
      "s_lshl_b32 s50, s81, 7\n"
      "s_add_u32 m0, m0, 8192\n"
      "s_add_u32 s50, s50, s79\n"
      "buffer_load_dwordx4 v224, s[8:11], s50 offen lds\n"
      "buffer_load_dwordx4 v225, s[8:11], s50 offen offset:1024 lds\n"
      "s_add_u32 s81, s81, 1\n"
      "s_waitcnt vmcnt(12)\n"
      "s_barrier\n"
      "s_and_b32 s81, s81, 7\n"
      "s_lshl_b32 s50, s81, 7\n"
      "s_add_u32 m0, m0, 8192\n"
      "s_add_u32 s50, s50, s79\n"
      "buffer_load_dwordx4 v224, s[8:11], s50 offen lds\n"
      "buffer_load_dwordx4 v225, s[8:11], s50 offen offset:1024 lds\n"
      "s_add_u32 s81, s81, 1\n"
      "s_and_b32 s81, s81, 7\n"
      "s_lshl_b32 s50, s81, 7\n"
      "s_add_u32 m0, m0, 8192\n"
      "s_add_u32 s50, s50, s79\n"
      "buffer_load_dwordx4 v224, s[8:11], s50 offen lds\n"
      "buffer_load_dwordx4 v225, s[8:11], s50 offen offset:1024 lds\n"
      "s_add_u32 s81, s81, 1\n"
      ".Ll_w2:\n"
      "v_readfirstlane_b32 s61, v7\n"
      "s_add_u32 s61, s61, 2654435769\n"
      "s_not_b32 s60, s61\n"
      "s_add_u32 m0, s84, 0\n"
      "s_nop 0\n"
      "buffer_load_dwordx4 v230, s[16:19], s24 offen offset:0 lds\n"
      "buffer_load_dwordx4 v231, s[16:19], s24 offen offset:1024 lds\n"
      "buffer_load_dwordx4 v232, s[16:19], s24 offen offset:2048 lds\n"
      "buffer_load_dwordx4 v233, s[16:19], s24 offen offset:3072 lds\n"
      "s_add_u32 m0, s84, 4096\n"
      "s_add_u32 s24, s24, 131072\n"
      "buffer_load_dwordx4 v230, s[16:19], s24 offen offset:0 lds\n"
      "buffer_load_dwordx4 v231, s[16:19], s24 offen offset:1024 lds\n"
      "buffer_load_dwordx4 v232, s[16:19], s24 offen offset:2048 lds\n"
      "buffer_load_dwordx4 v233, s[16:19], s24 offen offset:3072 lds\n"
      "s_add_u32 m0, s84, 32768\n"
      "s_nop 0\n"
      "buffer_load_dwordx4 v230, s[16:19], s25 offen offset:0 lds\n"
      "buffer_load_dwordx4 v231, s[16:19], s25 offen offset:1024 lds\n"
      "buffer_load_dwordx4 v232, s[16:19], s25 offen offset:2048 lds\n"
      "buffer_load_dwordx4 v233, s[16:19], s25 offen offset:3072 lds\n"
      "s_cmp_eq_u32 s48, 0\n"
      "s_cbranch_scc1 .Ll_xx\n"
      "s_waitcnt vmcnt(12)\n"
      "s_barrier\n"
      ".Ll_xx:\n"
      "s_add_u32 m0, s84, 36864\n"
      "s_add_u32 s25, s25, 131072\n"
      "s_waitcnt vmcnt(8)\n"
      "buffer_load_dwordx4 v230, s[16:19], s25 offen offset:0 lds\n"
      "ds_read_b64_tr_b16 v[8:9], v2 offset:0\n"
      "ds_read_b64_tr_b16 v[10:11], v2 offset:2048\n"
      "ds_read_b64_tr_b16 v[12:13], v3 offset:0\n"
      "buffer_load_dwordx4 v231, s[16:19], s25 offen offset:1024 lds\n"
      "ds_read_b64_tr_b16 v[14:15], v3 offset:2048\n"
      "ds_read_b64_tr_b16 v[16:17], v4 offset:0\n"
      "ds_read_b64_tr_b16 v[18:19], v4 offset:2048\n"
      "buffer_load_dwordx4 v232, s[16:19], s25 offen offset:2048 lds\n"
      "ds_read_b64_tr_b16 v[20:21], v5 offset:0\n"
      "ds_read_b64_tr_b16 v[22:23], v5 offset:2048\n"
      "buffer_load_dwordx4 v233, s[16:19], s25 offen offset:3072 lds\n"
      "s_waitcnt lgkmcnt(0)\n"
      "s_add_u32 m0, s84, 0\n"
      "s_waitcnt vmcnt(8)\n"
      "s_waitcnt vmcnt(12)\n"
      "buffer_load_dwordx4 v230, s[16:19], s26 offen offset:0 lds\n"
      "ds_read_b64_tr_b16 v[24:25], v2 offset:4096\n"
      "ds_read_b64_tr_b16 v[26:27], v2 offset:6144\n"
      "ds_read_b64_tr_b16 v[28:29], v3 offset:4096\n"
      "s_waitcnt vmcnt(12)\n"
      "buffer_load_dwordx4 v231, s[16:19], s26 offen offset:1024 lds\n"
      "ds_read_b64_tr_b16 v[30:31], v3 offset:6144\n"
      "ds_read_b64_tr_b16 v[32:33], v4 offset:4096\n"
      "ds_read_b64_tr_b16 v[34:35], v4 offset:6144\n"
      "s_waitcnt vmcnt(12)\n"
      "buffer_load_dwordx4 v232, s[16:19], s26 offen offset:2048 lds\n"
      "ds_read_b64_tr_b16 v[36:37], v5 offset:4096\n"
      "ds_read_b64_tr_b16 v[38:39], v5 offset:6144\n"
      "s_waitcnt vmcnt(12)\n"
      "buffer_load_dwordx4 v233, s[16:19], s26 offen offset:3072 lds\n"
      "s_waitcnt lgkmcnt(0)\n"
      "s_add_u32 m0, s84, 4096\n"
      "s_add_u32 s26, s26, 131072\n"
      "s_waitcnt vmcnt(8)\n"
      "s_waitcnt vmcnt(12)\n"
      "buffer_load_dwordx4 v230, s[16:19], s26 offen offset:0 lds\n"
      "ds_read_b64_tr_b16 v[40:41], v2 offset:32768\n"
      "ds_read_b64_tr_b16 v[42:43], v2 offset:34816\n"
      "ds_read_b64_tr_b16 v[44:45], v3 offset:32768\n"
      "s_waitcnt vmcnt(12)\n"
      "buffer_load_dwordx4 v231, s[16:19], s26 offen offset:1024 lds\n"
      "ds_read_b64_tr_b16 v[46:47], v3 offset:34816\n"
      "ds_read_b64_tr_b16 v[48:49], v4 offset:32768\n"
      "ds_read_b64_tr_b16 v[50:51], v4 offset:34816\n"
      "s_waitcnt vmcnt(12)\n"
      "buffer_load_dwordx4 v232, s[16:19], s26 offen offset:2048 lds\n"
      "ds_read_b64_tr_b16 v[52:53], v5 offset:32768\n"
      "ds_read_b64_tr_b16 v[54:55], v5 offset:34816\n"
      "s_waitcnt vmcnt(12)\n"
      "buffer_load_dwordx4 v233, s[16:19], s26 offen offset:3072 lds\n"
      "s_waitcnt lgkmcnt(0)\n"
      "s_add_u32 m0, s84, 32768\n"
      "s_waitcnt vmcnt(8)\n"
      "s_waitcnt vmcnt(12)\n"
      "buffer_load_dwordx4 v230, s[16:19], s27 offen offset:0 lds\n"
      "ds_read_b64_tr_b16 v[56:57], v2 offset:36864\n"
      "ds_read_b64_tr_b16 v[58:59], v2 offset:38912\n"
      "ds_read_b64_tr_b16 v[60:61], v3 offset:36864\n"
      "s_waitcnt vmcnt(12)\n"
      "buffer_load_dwordx4 v231, s[16:19], s27 offen offset:1024 lds\n"
      "ds_read_b64_tr_b16 v[62:63], v3 offset:38912\n"
      "ds_read_b64_tr_b16 v[64:65], v4 offset:36864\n"
      "ds_read_b64_tr_b16 v[66:67], v4 offset:38912\n"
      "s_waitcnt vmcnt(12)\n"
      "buffer_load_dwordx4 v232, s[16:19], s27 offen offset:2048 lds\n"
      "ds_read_b64_tr_b16 v[68:69], v5 offset:36864\n"
      "ds_read_b64_tr_b16 v[70:71], v5 offset:38912\n"
      "s_waitcnt vmcnt(12)\n"
      "buffer_load_dwordx4 v233, s[16:19], s27 offen offset:3072 lds\n"
      "s_waitcnt lgkmcnt(0)\n"
      "s_add_u32 m0, s84, 36864\n"
      "s_add_u32 s27, s27, 131072\n"
      "s_waitcnt vmcnt(8)\n"
      "s_waitcnt vmcnt(12)\n"
      "buffer_load_dwordx4 v230, s[16:19], s27 offen offset:0 lds\n"
      "ds_read_b64_tr_b16 v[72:73], v2 offset:0\n"
      "ds_read_b64_tr_b16 v[74:75], v2 offset:2048\n"
      "ds_read_b64_tr_b16 v[76:77], v3 offset:0\n"
      "s_waitcnt vmcnt(12)\n"
      "buffer_load_dwordx4 v231, s[16:19], s27 offen offset:1024 lds\n"
      "ds_read_b64_tr_b16 v[78:79], v3 offset:2048\n"
      "ds_read_b64_tr_b16 v[80:81], v4 offset:0\n"
      "ds_read_b64_tr_b16 v[82:83], v4 offset:2048\n"
      "s_waitcnt vmcnt(12)\n"
      "buffer_load_dwordx4 v232, s[16:19], s27 offen offset:2048 lds\n"
      "ds_read_b64_tr_b16 v[84:85], v5 offset:0\n"
      "ds_read_b64_tr_b16 v[86:87], v5 offset:2048\n"
      "s_waitcnt vmcnt(12)\n"
      "buffer_load_dwordx4 v233, s[16:19], s27 offen offset:3072 lds\n"
      "s_add_u32 m0, s84, 65536\n"
      "s_waitcnt vmcnt(8)\n"
      "s_waitcnt vmcnt(12)\n"
      "buffer_load_dwordx4 v230, s[16:19], s28 offen offset:0 lds\n"
      "ds_read_b64_tr_b16 v[88:89], v2 offset:4096\n"
      "ds_read_b64_tr_b16 v[90:91], v2 offset:6144\n"
      "ds_read_b64_tr_b16 v[92:93], v3 offset:4096\n"
      "s_waitcnt vmcnt(12)\n"
      "buffer_load_dwordx4 v231, s[16:19], s28 offen offset:1024 lds\n"
      "ds_read_b64_tr_b16 v[94:95], v3 offset:6144\n"
      "ds_read_b64_tr_b16 v[96:97], v4 offset:4096\n"
      "ds_read_b64_tr_b16 v[98:99], v4 offset:6144\n"
      "s_waitcnt vmcnt(12)\n"
      "buffer_load_dwordx4 v232, s[16:19], s28 offen offset:2048 lds\n"
      "ds_read_b64_tr_b16 v[100:101], v5 offset:4096\n"
      "ds_read_b64_tr_b16 v[102:103], v5 offset:6144\n"
      "s_waitcnt vmcnt(12)\n"
      "buffer_load_dwordx4 v233, s[16:19], s28 offen offset:3072 lds\n"
      "s_add_u32 m0, s84, 69632\n"
      "s_add_u32 s28, s28, 131072\n"
      "s_waitcnt vmcnt(8)\n"
      "s_waitcnt vmcnt(12)\n"
      "buffer_load_dwordx4 v230, s[16:19], s28 offen offset:0 lds\n"
      "ds_read_b64_tr_b16 v[104:105], v2 offset:32768\n"
      "ds_read_b64_tr_b16 v[106:107], v2 offset:34816\n"
      "ds_read_b64_tr_b16 v[108:109], v3 offset:32768\n"
      "s_waitcnt vmcnt(12)\n"
      "buffer_load_dwordx4 v231, s[16:19], s28 offen offset:1024 lds\n"
      "ds_read_b64_tr_b16 v[110:111], v3 offset:34816\n"
      "ds_read_b64_tr_b16 v[112:113], v4 offset:32768\n"
      "ds_read_b64_tr_b16 v[114:115], v4 offset:34816\n"
      "s_waitcnt vmcnt(12)\n"
      "buffer_load_dwordx4 v232, s[16:19], s28 offen offset:2048 lds\n"
      "ds_read_b64_tr_b16 v[116:117], v5 offset:32768\n"
      "ds_read_b64_tr_b16 v[118:119], v5 offset:34816\n"
      "s_waitcnt vmcnt(12)\n"
      "buffer_load_dwordx4 v233, s[16:19], s28 offen offset:3072 lds\n"
      "s_waitcnt lgkmcnt(15)\n"
      "s_add_u32 m0, s84, 0\n"
      "s_waitcnt vmcnt(8)\n"
      "s_waitcnt vmcnt(12)\n"
      "buffer_load_dwordx4 v230, s[16:19], s29 offen offset:0 lds\n"
      "ds_read_b64_tr_b16 v[120:121], v2 offset:36864\n"
      "ds_read_b64_tr_b16 v[122:123], v2 offset:38912\n"
      "ds_read_b64_tr_b16 v[124:125], v3 offset:36864\n"
      "s_waitcnt vmcnt(12)\n"
      "buffer_load_dwordx4 v231, s[16:19], s29 offen offset:1024 lds\n"
      "ds_read_b64_tr_b16 v[126:127], v3 offset:38912\n"
      "ds_read_b64_tr_b16 v[128:129], v4 offset:36864\n"
      "ds_read_b64_tr_b16 v[130:131], v4 offset:38912\n"
      "s_waitcnt vmcnt(12)\n"
      "buffer_load_dwordx4 v232, s[16:19], s29 offen offset:2048 lds\n"
      "ds_read_b64_tr_b16 v[132:133], v5 offset:36864\n"
      "ds_read_b64_tr_b16 v[134:135], v5 offset:38912\n"
      "s_waitcnt vmcnt(12)\n"
      "buffer_load_dwordx4 v233, s[16:19], s29 offen offset:3072 lds\n"
      "s_waitcnt lgkmcnt(15)\n"
      "s_add_u32 m0, s84, 4096\n"
      "s_add_u32 s29, s29, 131072\n"
      "s_waitcnt vmcnt(12)\n"
      "buffer_load_dwordx4 v230, s[16:19], s29 offen offset:0 lds\n"
      "s_waitcnt vmcnt(12)\n"
      "buffer_load_dwordx4 v231, s[16:19], s29 offen offset:1024 lds\n"
      "s_waitcnt vmcnt(12)\n"
      "buffer_load_dwordx4 v232, s[16:19], s29 offen offset:2048 lds\n"
      "s_waitcnt vmcnt(12)\n"
      "buffer_load_dwordx4 v233, s[16:19], s29 offen offset:3072 lds\n"
      "s_waitcnt lgkmcnt(8)\n"
      "s_add_u32 m0, s84, 32768\n"
      "s_nop 0\n"
      "s_waitcnt vmcnt(12)\n"
      "buffer_load_dwordx4 v230, s[16:19], s30 offen offset:0 lds\n"
      "s_waitcnt vmcnt(12)\n"
      "buffer_load_dwordx4 v231, s[16:19], s30 offen offset:1024 lds\n"
      "s_waitcnt vmcnt(12)\n"
      "buffer_load_dwordx4 v232, s[16:19], s30 offen offset:2048 lds\n"
      "s_waitcnt vmcnt(12)\n"
      "buffer_load_dwordx4 v233, s[16:19], s30 offen offset:3072 lds\n"
      "s_waitcnt lgkmcnt(0)\n"
      "s_add_u32 m0, s84, 36864\n"
      "s_add_u32 s30, s30, 131072\n"
      "s_cmp_eq_u32 s48, 0\n"
      "s_cbranch_scc1 .Ll_lk\n"
      "v_min_u32 v238, 13, v1\n"
      "v_lshlrev_b32 v238, 3, v238\n"
      "buffer_load_dwordx2 v[238:239], v238, s[20:23], s87 offen offset:512 sc1\n"
      ".Ll_lk:\n"
      "s_waitcnt vmcnt(12)\n"
      "buffer_load_dwordx4 v230, s[16:19], s30 offen offset:0 lds\n"
      "s_waitcnt vmcnt(12)\n"
      "buffer_load_dwordx4 v231, s[16:19], s30 offen offset:1024 lds\n"
      "s_waitcnt vmcnt(12)\n"
      "buffer_load_dwordx4 v232, s[16:19], s30 offen offset:2048 lds\n"
      "s_waitcnt vmcnt(12)\n"
      "buffer_load_dwordx4 v233, s[16:19], s30 offen offset:3072 lds\n"
      "s_cmp_eq_u32 s48, 1\n"
      "s_cbranch_scc1 .Ll_p2\n"
      ".Ll_pause:\n"
      "s_sleep 2\n"
      "s_memtime s[6:7]\n"
      "s_waitcnt lgkmcnt(0)\n"
      "s_sub_u32 s6, s6, s4\n"
      "s_cmp_lt_u32 s6, 12000\n"
      "s_cbranch_scc1 .Ll_pause\n"
      "v_min_u32 v238, 13, v1\n"
      "v_lshlrev_b32 v238, 3, v238\n"
      "buffer_load_dwordx2 v[238:239], v238, s[20:23], s87 offen offset:512 sc1\n"
      "s_waitcnt vmcnt(0)\n"
      "s_branch .Ll_p2\n"
      ".p2align 12\n"
      ".Lc_stats:\n"
      "s_waitcnt vmcnt(3)\n"
      "v_readfirstlane_b32 s60, v32\n"
      "v_readfirstlane_b32 s61, v33\n"
      "v_lshlrev_b32 v64, 16, v48\n"
      "v_and_b32 v65, s58, v48\n"
      "v_lshlrev_b32 v66, 16, v49\n"
      "v_and_b32 v67, s58, v49\n"
      "v_lshlrev_b32 v68, 16, v50\n"
      "v_and_b32 v69, s58, v50\n"
      "v_lshlrev_b32 v70, 16, v51\n"
      "v_and_b32 v71, s58, v51\n"
      "v_lshlrev_b32 v72, 16, v52\n"
      "v_and_b32 v73, s58, v52\n"
      "v_lshlrev_b32 v74, 16, v53\n"
      "v_and_b32 v75, s58, v53\n"
      "v_lshlrev_b32 v76, 16, v54\n"
      "v_and_b32 v77, s58, v54\n"
      "v_lshlrev_b32 v78, 16, v55\n"
      "v_and_b32 v79, s58, v55\n"
      "v_pk_add_f32 v[56:57], v[64:65], v[66:67]\n"
      "v_pk_add_f32 v[58:59], v[68:69], v[70:71]\n"
      "v_pk_add_f32 v[60:61], v[72:73], v[74:75]\n"
      "v_pk_add_f32 v[62:63], v[76:77], v[78:79]\n"
      "v_pk_add_f32 v[56:57], v[56:57], v[58:59]\n"
      "v_pk_add_f32 v[60:61], v[60:61], v[62:63]\n"
      "v_pk_add_f32 v[56:57], v[56:57], v[60:61]\n"
      "v_add_f32 v56, v56, v57\n"
      "s_nop 1\n"
      "v_add_f32_dpp v56, v56, v56 quad_perm:[1,0,3,2] row_mask:0xf bank_mask:0xf bound_ctrl:1\n"
      "s_nop 1\n"
      "v_add_f32_dpp v56, v56, v56 quad_perm:[2,3,0,1] row_mask:0xf bank_mask:0xf bound_ctrl:1\n"
      "s_nop 1\n"
      "v_add_f32_dpp v56, v56, v56 row_half_mirror row_mask:0xf bank_mask:0xf bound_ctrl:1\n"
      "s_nop 1\n"
      "v_add_f32_dpp v56, v56, v56 row_mirror row_mask:0xf bank_mask:0xf bound_ctrl:1\n"
      "v_mov_b32 v57, v56\n"
      "s_nop 1\n"
      "v_permlane16_swap_b32 v56, v57\n"
      "v_add_f32 v56, v56, v57\n"
      "v_mul_f32 v56, 0xbb000000, v56\n"
      "v_pk_add_f32 v[64:65], v[64:65], v[56:57] op_sel_hi:[1,0]\n"
      "v_pk_add_f32 v[66:67], v[66:67], v[56:57] op_sel_hi:[1,0]\n"
      "v_pk_add_f32 v[68:69], v[68:69], v[56:57] op_sel_hi:[1,0]\n"
      "v_pk_add_f32 v[70:71], v[70:71], v[56:57] op_sel_hi:[1,0]\n"
      "v_pk_add_f32 v[72:73], v[72:73], v[56:57] op_sel_hi:[1,0]\n"
      "v_pk_add_f32 v[74:75], v[74:75], v[56:57] op_sel_hi:[1,0]\n"
      "v_pk_add_f32 v[76:77], v[76:77], v[56:57] op_sel_hi:[1,0]\n"
      "v_pk_add_f32 v[78:79], v[78:79], v[56:57] op_sel_hi:[1,0]\n"
      "v_pk_mul_f32 v[58:59], v[64:65], v[64:65]\n"
      "v_pk_mul_f32 v[60:61], v[66:67], v[66:67]\n"
      "v_pk_fma_f32 v[58:59], v[68:69], v[68:69], v[58:59]\n"
      "v_pk_fma_f32 v[60:61], v[70:71], v[70:71], v[60:61]\n"
      "v_pk_fma_f32 v[58:59], v[72:73], v[72:73], v[58:59]\n"
      "v_pk_fma_f32 v[60:61], v[74:75], v[74:75], v[60:61]\n"
      "v_pk_fma_f32 v[58:59], v[76:77], v[76:77], v[58:59]\n"
      "v_pk_fma_f32 v[60:61], v[78:79], v[78:79], v[60:61]\n"
      "v_pk_add_f32 v[58:59], v[58:59], v[60:61]\n"
      "v_add_f32 v58, v58, v59\n"
      "s_nop 1\n"
      "v_add_f32_dpp v58, v58, v58 quad_perm:[1,0,3,2] row_mask:0xf bank_mask:0xf bound_ctrl:1\n"
      "s_nop 1\n"
      "v_add_f32_dpp v58, v58, v58 quad_perm:[2,3,0,1] row_mask:0xf bank_mask:0xf bound_ctrl:1\n"
      "s_nop 1\n"
      "v_add_f32_dpp v58, v58, v58 row_half_mirror row_mask:0xf bank_mask:0xf bound_ctrl:1\n"
      "s_nop 1\n"
      "v_add_f32_dpp v58, v58, v58 row_mirror row_mask:0xf bank_mask:0xf bound_ctrl:1\n"
      "v_mov_b32 v59, v58\n"
      "s_nop 1\n"
      "v_permlane16_swap_b32 v58, v59\n"
      "v_add_f32 v58, v58, v59\n"
      "v_mov_b32 v59, 0x3727c5ac\n"
      "v_fmac_f32 v59, 0x3b000000, v58\n"
      "v_sqrt_f32 v57, v59\n"
      "s_waitcnt lgkmcnt(0)\n"
      "s_add_u32 s61, s61, 2654435769\n"
      "s_add_u32 s60, s60, 2654435769\n"
      "v_cvt_pk_bf16_f32 v64, v56, v57\n"
      "v_lshlrev_b32 v58, 16, v64\n"
      "v_and_b32 v59, s58, v64\n"
      "v_pk_add_f32 v[56:57], v[56:57], v[58:59] neg_lo:[0,1] neg_hi:[0,1]\n"
      "v_cvt_pk_bf16_f32 v65, v56, v57\n"
      "v_perm_b32 v28, v65, v64, s50\n"
      "v_perm_b32 v30, v65, v64, s51\n"
      "v_mov_b32 v29, s61\n"
      "v_mov_b32 v31, s61\n"
      "v_lshrrev_b32 v24, 1, v1\n"
      "s_cmp_eq_u32 s48, 0\n"
      "s_mov_b32 exec_lo, 1\n"
      "s_mov_b32 exec_hi, 1\n"
      "s_barrier\n"
      "buffer_store_dwordx4 v[28:31], v24, s[20:23], s53 offen sc1\n"
      "s_mov_b64 exec, -1\n"
      "s_cbranch_scc1 .Lc_fin\n"
      "s_mov_b32 s82, 65536\n"
      "ds_read_b64_tr_b16 v[124:125], v2 offset:0\n"
      "ds_read_b64_tr_b16 v[126:127], v2 offset:1024\n"
      "ds_read_b64_tr_b16 v[128:129], v3 offset:0\n"
      "ds_read_b64_tr_b16 v[130:131], v3 offset:1024\n"
      "ds_read_b128 v[48:51], v4 offset:0\n"
      "ds_read_b128 v[52:55], v4 offset:2048\n"
      "ds_read_b128 v[56:59], v4 offset:4096\n"
      "ds_read_b128 v[60:63], v4 offset:6144\n"
      "s_waitcnt lgkmcnt(0)\n"
      "v_mfma_f32_16x16x32_bf16 v[80:83], v[124:127], v[48:51], 0\n"
      "ds_read_b64_tr_b16 v[132:133], v2 offset:8192\n"
      "ds_read_b64_tr_b16 v[134:135], v2 offset:9216\n"
      "v_mfma_f32_16x16x32_bf16 v[84:87], v[128:131], v[48:51], 0\n"
      "ds_read_b128 v[64:67], v5 offset:0\n"
      "ds_read_b64_tr_b16 v[136:137], v3 offset:8192\n"
      "v_mfma_f32_16x16x32_bf16 v[88:91], v[124:127], v[52:55], 0\n"
      "ds_read_b64_tr_b16 v[138:139], v3 offset:9216\n"
      "ds_read_b128 v[68:71], v5 offset:2048\n"
      "v_mfma_f32_16x16x32_bf16 v[92:95], v[128:131], v[52:55], 0\n"
      "ds_read_b128 v[72:75], v5 offset:4096\n"
      "ds_read_b128 v[76:79], v5 offset:6144\n"
      "v_mfma_f32_16x16x32_bf16 v[96:99], v[124:127], v[56:59], 0\n"
      "v_mfma_f32_16x16x32_bf16 v[100:103], v[128:131], v[56:59], 0\n"
      "v_mfma_f32_16x16x32_bf16 v[104:107], v[124:127], v[60:63], 0\n"
      "v_mfma_f32_16x16x32_bf16 v[108:111], v[128:131], v[60:63], 0\n"
      "v_mfma_f32_16x16x32_bf16 v[16:19], v[12:15], v[124:127], 0\n"
      "v_mfma_f32_16x16x32_bf16 v[20:23], v[12:15], v[128:131], 0\n"
      "s_waitcnt lgkmcnt(0)\n"
      "v_mfma_f32_16x16x32_bf16 v[80:83], v[132:135], v[64:67], v[80:83]\n"
      "v_mfma_f32_16x16x32_bf16 v[84:87], v[136:139], v[64:67], v[84:87]\n"
      "s_barrier\n"
      "ds_read_b64_tr_b16 v[140:141], v2 offset:24576\n"
      "ds_read_b64_tr_b16 v[142:143], v2 offset:25600\n"
      "v_mfma_f32_16x16x32_bf16 v[88:91], v[132:135], v[68:71], v[88:91]\n"
      "ds_read_b128 v[48:51], v4 offset:24576\n"
      "ds_read_b64_tr_b16 v[144:145], v3 offset:24576\n"
      "v_mfma_f32_16x16x32_bf16 v[92:95], v[136:139], v[68:71], v[92:95]\n"
      "ds_read_b64_tr_b16 v[146:147], v3 offset:25600\n"
      "ds_read_b128 v[52:55], v4 offset:26624\n"
      "v_mfma_f32_16x16x32_bf16 v[96:99], v[132:135], v[72:75], v[96:99]\n"
      "ds_read_b128 v[56:59], v4 offset:28672\n"
      "ds_read_b128 v[60:63], v4 offset:30720\n"
      "v_mfma_f32_16x16x32_bf16 v[100:103], v[136:139], v[72:75], v[100:103]\n"
      "v_mfma_f32_16x16x32_bf16 v[104:107], v[132:135], v[76:79], v[104:107]\n"
      "v_mfma_f32_16x16x32_bf16 v[108:111], v[136:139], v[76:79], v[108:111]\n"
      "v_mfma_f32_16x16x32_bf16 v[16:19], v[12:15], v[132:135], v[16:19]\n"
      "v_mfma_f32_16x16x32_bf16 v[20:23], v[12:15], v[136:139], v[20:23]\n"
      "s_waitcnt lgkmcnt(0)\n"
      "v_mfma_f32_16x16x32_bf16 v[80:83], v[140:143], v[48:51], v[80:83]\n"
      "ds_read_b64_tr_b16 v[148:149], v2 offset:32768\n"
      "ds_read_b64_tr_b16 v[150:151], v2 offset:33792\n"
      "v_mfma_f32_16x16x32_bf16 v[84:87], v[144:147], v[48:51], v[84:87]\n"
      "ds_read_b128 v[64:67], v5 offset:24576\n"
      "ds_read_b64_tr_b16 v[152:153], v3 offset:32768\n"
      "v_mfma_f32_16x16x32_bf16 v[88:91], v[140:143], v[52:55], v[88:91]\n"
      "ds_read_b64_tr_b16 v[154:155], v3 offset:33792\n"
      "ds_read_b128 v[68:71], v5 offset:26624\n"
      "v_mfma_f32_16x16x32_bf16 v[92:95], v[144:147], v[52:55], v[92:95]\n"
      "ds_read_b128 v[72:75], v5 offset:28672\n"
      "ds_read_b128 v[76:79], v5 offset:30720\n"
      "v_mfma_f32_16x16x32_bf16 v[96:99], v[140:143], v[56:59], v[96:99]\n"
      "v_mfma_f32_16x16x32_bf16 v[100:103], v[144:147], v[56:59], v[100:103]\n"
      "v_mfma_f32_16x16x32_bf16 v[104:107], v[140:143], v[60:63], v[104:107]\n"
      "v_mfma_f32_16x16x32_bf16 v[108:111], v[144:147], v[60:63], v[108:111]\n"
      "v_mfma_f32_16x16x32_bf16 v[16:19], v[12:15], v[140:143], v[16:19]\n"
      "v_mfma_f32_16x16x32_bf16 v[20:23], v[12:15], v[144:147], v[20:23]\n"
      "s_waitcnt lgkmcnt(0)\n"
      "v_mfma_f32_16x16x32_bf16 v[80:83], v[148:151], v[64:67], v[80:83]\n"
      "v_mfma_f32_16x16x32_bf16 v[84:87], v[152:155], v[64:67], v[84:87]\n"
      "s_barrier\n"
      "ds_read_b64_tr_b16 v[156:157], v116 offset:0\n"
      "ds_read_b64_tr_b16 v[158:159], v116 offset:1024\n"
      "v_mfma_f32_16x16x32_bf16 v[88:91], v[148:151], v[68:71], v[88:91]\n"
      "ds_read_b128 v[48:51], v118 offset:0\n"
      "ds_read_b64_tr_b16 v[160:161], v117 offset:0\n"
      "v_mfma_f32_16x16x32_bf16 v[92:95], v[152:155], v[68:71], v[92:95]\n"
      "ds_read_b64_tr_b16 v[162:163], v117 offset:1024\n"
      "ds_read_b128 v[52:55], v118 offset:2048\n"
      "v_mfma_f32_16x16x32_bf16 v[96:99], v[148:151], v[72:75], v[96:99]\n"
      "ds_read_b128 v[56:59], v118 offset:4096\n"
      "ds_read_b128 v[60:63], v118 offset:6144\n"
      "v_mfma_f32_16x16x32_bf16 v[100:103], v[152:155], v[72:75], v[100:103]\n"
      "v_mfma_f32_16x16x32_bf16 v[104:107], v[148:151], v[76:79], v[104:107]\n"
      "v_mfma_f32_16x16x32_bf16 v[108:111], v[152:155], v[76:79], v[108:111]\n"
      "v_mfma_f32_16x16x32_bf16 v[16:19], v[12:15], v[148:151], v[16:19]\n"
      "v_mfma_f32_16x16x32_bf16 v[20:23], v[12:15], v[152:155], v[20:23]\n"
      "s_waitcnt lgkmcnt(0)\n"
      "v_mfma_f32_16x16x32_bf16 v[80:83], v[156:159], v[48:51], v[80:83]\n"
      "ds_read_b64_tr_b16 v[164:165], v116 offset:8192\n"
      "ds_read_b64_tr_b16 v[166:167], v116 offset:9216\n"
      "v_mfma_f32_16x16x32_bf16 v[84:87], v[160:163], v[48:51], v[84:87]\n"
      "ds_read_b128 v[64:67], v119 offset:0\n"
      "ds_read_b64_tr_b16 v[168:169], v117 offset:8192\n"
      "v_mfma_f32_16x16x32_bf16 v[88:91], v[156:159], v[52:55], v[88:91]\n"
      "ds_read_b64_tr_b16 v[170:171], v117 offset:9216\n"
      "ds_read_b128 v[68:71], v119 offset:2048\n"
      "v_mfma_f32_16x16x32_bf16 v[92:95], v[160:163], v[52:55], v[92:95]\n"
      "ds_read_b128 v[72:75], v119 offset:4096\n"
      "ds_read_b128 v[76:79], v119 offset:6144\n"
      "v_mfma_f32_16x16x32_bf16 v[96:99], v[156:159], v[56:59], v[96:99]\n"
      "v_mfma_f32_16x16x32_bf16 v[100:103], v[160:163], v[56:59], v[100:103]\n"
      "v_mfma_f32_16x16x32_bf16 v[104:107], v[156:159], v[60:63], v[104:107]\n"
      "v_mfma_f32_16x16x32_bf16 v[108:111], v[160:163], v[60:63], v[108:111]\n"
      "v_mfma_f32_16x16x32_bf16 v[16:19], v[12:15], v[156:159], v[16:19]\n"
      "v_mfma_f32_16x16x32_bf16 v[20:23], v[12:15], v[160:163], v[20:23]\n"
      "s_add_u32 s4, s62, .Lpad_b2-.Lstart_k-4\n"
      "s_addc_u32 s5, s63, 0\n"
      "s_swappc_b64 s[6:7], s[4:5]\n"
      "s_waitcnt lgkmcnt(0)\n"
      "v_mfma_f32_16x16x32_bf16 v[80:83], v[164:167], v[64:67], v[80:83]\n"
      "v_mfma_f32_16x16x32_bf16 v[84:87], v[168:171], v[64:67], v[84:87]\n"
      "s_barrier\n"
      "ds_read_b64_tr_b16 v[172:173], v116 offset:24576\n"
      "ds_read_b64_tr_b16 v[174:175], v116 offset:25600\n"
      "v_mfma_f32_16x16x32_bf16 v[88:91], v[164:167], v[68:71], v[88:91]\n"
      "ds_read_b128 v[48:51], v118 offset:24576\n"
      "ds_read_b64_tr_b16 v[176:177], v117 offset:24576\n"
      "v_mfma_f32_16x16x32_bf16 v[92:95], v[168:171], v[68:71], v[92:95]\n"
      "ds_read_b64_tr_b16 v[178:179], v117 offset:25600\n"
      "ds_read_b128 v[52:55], v118 offset:26624\n"
      "v_mfma_f32_16x16x32_bf16 v[96:99], v[164:167], v[72:75], v[96:99]\n"
      "ds_read_b128 v[56:59], v118 offset:28672\n"
      "ds_read_b128 v[60:63], v118 offset:30720\n"
      "v_mfma_f32_16x16x32_bf16 v[100:103], v[168:171], v[72:75], v[100:103]\n"
      "v_mfma_f32_16x16x32_bf16 v[104:107], v[164:167], v[76:79], v[104:107]\n"
      "v_mfma_f32_16x16x32_bf16 v[108:111], v[168:171], v[76:79], v[108:111]\n"
      "v_mfma_f32_16x16x32_bf16 v[16:19], v[12:15], v[164:167], v[16:19]\n"
      "v_mfma_f32_16x16x32_bf16 v[20:23], v[12:15], v[168:171], v[20:23]\n"
      "s_waitcnt lgkmcnt(0)\n"
      "v_mfma_f32_16x16x32_bf16 v[80:83], v[172:175], v[48:51], v[80:83]\n"
      "ds_read_b64_tr_b16 v[180:181], v116 offset:32768\n"
      "ds_read_b64_tr_b16 v[182:183], v116 offset:33792\n"
      "v_mfma_f32_16x16x32_bf16 v[84:87], v[176:179], v[48:51], v[84:87]\n"
      "ds_read_b128 v[64:67], v119 offset:24576\n"
      "ds_read_b64_tr_b16 v[184:185], v117 offset:32768\n"
      "v_mfma_f32_16x16x32_bf16 v[88:91], v[172:175], v[52:55], v[88:91]\n"
      "ds_read_b64_tr_b16 v[186:187], v117 offset:33792\n"
      "ds_read_b128 v[68:71], v119 offset:26624\n"
      "v_mfma_f32_16x16x32_bf16 v[92:95], v[176:179], v[52:55], v[92:95]\n"
      "ds_read_b128 v[72:75], v119 offset:28672\n"
      "ds_read_b128 v[76:79], v119 offset:30720\n"
      "v_mfma_f32_16x16x32_bf16 v[96:99], v[172:175], v[56:59], v[96:99]\n"
      "v_mfma_f32_16x16x32_bf16 v[100:103], v[176:179], v[56:59], v[100:103]\n"
      "v_mfma_f32_16x16x32_bf16 v[104:107], v[172:175], v[60:63], v[104:107]\n"
      "v_mfma_f32_16x16x32_bf16 v[108:111], v[176:179], v[60:63], v[108:111]\n"
      "v_mfma_f32_16x16x32_bf16 v[16:19], v[12:15], v[172:175], v[16:19]\n"
      "v_mfma_f32_16x16x32_bf16 v[20:23], v[12:15], v[176:179], v[20:23]\n"
      "buffer_load_dwordx4 v[120:123], v7, s[20:23], 0 offen offset:0 sc1\n"
      "s_waitcnt lgkmcnt(0)\n"
      "v_mfma_f32_16x16x32_bf16 v[80:83], v[180:183], v[64:67], v[80:83]\n"
      "v_mfma_f32_16x16x32_bf16 v[84:87], v[184:187], v[64:67], v[84:87]\n"
      "s_barrier\n"
      "ds_read_b64_tr_b16 v[188:189], v2 offset:0\n"
      "ds_read_b64_tr_b16 v[190:191], v2 offset:1024\n"
      "v_mfma_f32_16x16x32_bf16 v[88:91], v[180:183], v[68:71], v[88:91]\n"
      "ds_read_b128 v[48:51], v4 offset:0\n"
      "ds_read_b64_tr_b16 v[192:193], v3 offset:0\n"
      "v_mfma_f32_16x16x32_bf16 v[92:95], v[184:187], v[68:71], v[92:95]\n"
      "ds_read_b64_tr_b16 v[194:195], v3 offset:1024\n"
      "ds_read_b128 v[52:55], v4 offset:2048\n"
      "v_mfma_f32_16x16x32_bf16 v[96:99], v[180:183], v[72:75], v[96:99]\n"
      "ds_read_b128 v[56:59], v4 offset:4096\n"
      "ds_read_b128 v[60:63], v4 offset:6144\n"
      "v_mfma_f32_16x16x32_bf16 v[100:103], v[184:187], v[72:75], v[100:103]\n"
      "v_mfma_f32_16x16x32_bf16 v[104:107], v[180:183], v[76:79], v[104:107]\n"
      "v_mfma_f32_16x16x32_bf16 v[108:111], v[184:187], v[76:79], v[108:111]\n"
      "v_mfma_f32_16x16x32_bf16 v[16:19], v[12:15], v[180:183], v[16:19]\n"
      "v_mfma_f32_16x16x32_bf16 v[20:23], v[12:15], v[184:187], v[20:23]\n"
      "s_waitcnt lgkmcnt(0)\n"
      "v_mfma_f32_16x16x32_bf16 v[80:83], v[188:191], v[48:51], v[80:83]\n"
      "ds_read_b64_tr_b16 v[196:197], v2 offset:8192\n"
      "ds_read_b64_tr_b16 v[198:199], v2 offset:9216\n"
      "v_mfma_f32_16x16x32_bf16 v[84:87], v[192:195], v[48:51], v[84:87]\n"
      "ds_read_b128 v[64:67], v5 offset:0\n"
      "ds_read_b64_tr_b16 v[200:201], v3 offset:8192\n"
      "v_mfma_f32_16x16x32_bf16 v[88:91], v[188:191], v[52:55], v[88:91]\n"
      "ds_read_b64_tr_b16 v[202:203], v3 offset:9216\n"
      "ds_read_b128 v[68:71], v5 offset:2048\n"
      "v_mfma_f32_16x16x32_bf16 v[92:95], v[192:195], v[52:55], v[92:95]\n"
      "ds_read_b128 v[72:75], v5 offset:4096\n"
      "ds_read_b128 v[76:79], v5 offset:6144\n"
      "v_mfma_f32_16x16x32_bf16 v[96:99], v[188:191], v[56:59], v[96:99]\n"
      "v_mfma_f32_16x16x32_bf16 v[100:103], v[192:195], v[56:59], v[100:103]\n"
      "v_mfma_f32_16x16x32_bf16 v[104:107], v[188:191], v[60:63], v[104:107]\n"
      "v_mfma_f32_16x16x32_bf16 v[108:111], v[192:195], v[60:63], v[108:111]\n"
      "v_mfma_f32_16x16x32_bf16 v[16:19], v[12:15], v[188:191], v[16:19]\n"
      "v_mfma_f32_16x16x32_bf16 v[20:23], v[12:15], v[192:195], v[20:23]\n"
      "s_add_u32 s4, s62, .Lpad_p2-.Lstart_k-4\n"
      "s_addc_u32 s5, s63, 0\n"
      "s_swappc_b64 s[6:7], s[4:5]\n"
      "s_waitcnt lgkmcnt(0)\n"
      "v_mfma_f32_16x16x32_bf16 v[80:83], v[196:199], v[64:67], v[80:83]\n"
      "v_mfma_f32_16x16x32_bf16 v[84:87], v[200:203], v[64:67], v[84:87]\n"
      "s_barrier\n"
      "ds_read_b64_tr_b16 v[204:205], v2 offset:24576\n"
      "ds_read_b64_tr_b16 v[206:207], v2 offset:25600\n"
      "v_mfma_f32_16x16x32_bf16 v[88:91], v[196:199], v[68:71], v[88:91]\n"
      "ds_read_b128 v[48:51], v4 offset:24576\n"
      "ds_read_b64_tr_b16 v[208:209], v3 offset:24576\n"
      "v_mfma_f32_16x16x32_bf16 v[92:95], v[200:203], v[68:71], v[92:95]\n"
      "ds_read_b64_tr_b16 v[210:211], v3 offset:25600\n"
      "ds_read_b128 v[52:55], v4 offset:26624\n"
      "v_mfma_f32_16x16x32_bf16 v[96:99], v[196:199], v[72:75], v[96:99]\n"
      "ds_read_b128 v[56:59], v4 offset:28672\n"
      "ds_read_b128 v[60:63], v4 offset:30720\n"
      "v_mfma_f32_16x16x32_bf16 v[100:103], v[200:203], v[72:75], v[100:103]\n"
      "v_mfma_f32_16x16x32_bf16 v[104:107], v[196:199], v[76:79], v[104:107]\n"
      "v_mfma_f32_16x16x32_bf16 v[108:111], v[200:203], v[76:79], v[108:111]\n"
      "v_mfma_f32_16x16x32_bf16 v[16:19], v[12:15], v[196:199], v[16:19]\n"
      "v_mfma_f32_16x16x32_bf16 v[20:23], v[12:15], v[200:203], v[20:23]\n"
      "s_waitcnt lgkmcnt(0)\n"
      "v_mfma_f32_16x16x32_bf16 v[80:83], v[204:207], v[48:51], v[80:83]\n"
      "ds_read_b64_tr_b16 v[212:213], v2 offset:32768\n"
      "ds_read_b64_tr_b16 v[214:215], v2 offset:33792\n"
      "v_mfma_f32_16x16x32_bf16 v[84:87], v[208:211], v[48:51], v[84:87]\n"
      "ds_read_b128 v[64:67], v5 offset:24576\n"
      "ds_read_b64_tr_b16 v[216:217], v3 offset:32768\n"
      "v_mfma_f32_16x16x32_bf16 v[88:91], v[204:207], v[52:55], v[88:91]\n"
      "ds_read_b64_tr_b16 v[218:219], v3 offset:33792\n"
      "ds_read_b128 v[68:71], v5 offset:26624\n"
      "v_mfma_f32_16x16x32_bf16 v[92:95], v[208:211], v[52:55], v[92:95]\n"
      "ds_read_b128 v[72:75], v5 offset:28672\n"
      "ds_read_b128 v[76:79], v5 offset:30720\n"
      "v_mfma_f32_16x16x32_bf16 v[96:99], v[204:207], v[56:59], v[96:99]\n"
      "v_mfma_f32_16x16x32_bf16 v[100:103], v[208:211], v[56:59], v[100:103]\n"
      "v_mfma_f32_16x16x32_bf16 v[104:107], v[204:207], v[60:63], v[104:107]\n"
      "v_mfma_f32_16x16x32_bf16 v[108:111], v[208:211], v[60:63], v[108:111]\n"
      "v_mfma_f32_16x16x32_bf16 v[16:19], v[12:15], v[204:207], v[16:19]\n"
      "v_mfma_f32_16x16x32_bf16 v[20:23], v[12:15], v[208:211], v[20:23]\n"
      "s_waitcnt lgkmcnt(0)\n"
      "v_mfma_f32_16x16x32_bf16 v[80:83], v[212:215], v[64:67], v[80:83]\n"
      "v_mfma_f32_16x16x32_bf16 v[84:87], v[216:219], v[64:67], v[84:87]\n"
      "s_barrier\n"
      "ds_read_b64_tr_b16 v[220:221], v116 offset:0\n"
      "ds_read_b64_tr_b16 v[222:223], v116 offset:1024\n"
      "v_mfma_f32_16x16x32_bf16 v[88:91], v[212:215], v[68:71], v[88:91]\n"
      "ds_read_b128 v[48:51], v118 offset:0\n"
      "ds_read_b64_tr_b16 v[224:225], v117 offset:0\n"
      "v_mfma_f32_16x16x32_bf16 v[92:95], v[216:219], v[68:71], v[92:95]\n"
      "ds_read_b64_tr_b16 v[226:227], v117 offset:1024\n"
      "ds_read_b128 v[52:55], v118 offset:2048\n"
      "v_mfma_f32_16x16x32_bf16 v[96:99], v[212:215], v[72:75], v[96:99]\n"
      "ds_read_b128 v[56:59], v118 offset:4096\n"
      "ds_read_b128 v[60:63], v118 offset:6144\n"
      "v_mfma_f32_16x16x32_bf16 v[100:103], v[216:219], v[72:75], v[100:103]\n"
      "v_mfma_f32_16x16x32_bf16 v[104:107], v[212:215], v[76:79], v[104:107]\n"
      "v_mfma_f32_16x16x32_bf16 v[108:111], v[216:219], v[76:79], v[108:111]\n"
      "v_mfma_f32_16x16x32_bf16 v[16:19], v[12:15], v[212:215], v[16:19]\n"
      "v_mfma_f32_16x16x32_bf16 v[20:23], v[12:15], v[216:219], v[20:23]\n"
      "s_waitcnt vmcnt(0)\n"
      ".Lc_gchk0:\n"
      "v_cmp_ne_u32 s[76:77], s60, v121\n"
      "v_cmp_ne_u32 vcc, s60, v123\n"
      "s_or_b64 vcc, vcc, s[76:77]\n"
      "s_cbranch_scc1 .Lc_gretry0\n"
      ".Lc_gok0:\n"
      "s_waitcnt lgkmcnt(0)\n"
      "v_mfma_f32_16x16x32_bf16 v[80:83], v[220:223], v[48:51], v[80:83]\n"
      "ds_read_b64_tr_b16 v[228:229], v116 offset:8192\n"
      "ds_read_b64_tr_b16 v[230:231], v116 offset:9216\n"
      "v_mfma_f32_16x16x32_bf16 v[84:87], v[224:227], v[48:51], v[84:87]\n"
      "ds_read_b128 v[64:67], v119 offset:0\n"
      "ds_read_b64_tr_b16 v[232:233], v117 offset:8192\n"
      "v_mfma_f32_16x16x32_bf16 v[88:91], v[220:223], v[52:55], v[88:91]\n"
      "ds_read_b64_tr_b16 v[234:235], v117 offset:9216\n"
      "ds_read_b128 v[68:71], v119 offset:2048\n"
      "v_mfma_f32_16x16x32_bf16 v[92:95], v[224:227], v[52:55], v[92:95]\n"
      "ds_read_b128 v[72:75], v119 offset:4096\n"
      "ds_read_b128 v[76:79], v119 offset:6144\n"
      "v_mfma_f32_16x16x32_bf16 v[96:99], v[220:223], v[56:59], v[96:99]\n"
      "v_lshl_or_b32 v8, v8, 16, v8\n"
      "v_cndmask_b32 v25, 0, v8, s[70:71]\n"
      "v_lshl_or_b32 v9, v9, 16, v9\n"
      "v_cndmask_b32 v27, 0, v9, s[70:71]\n"
      "v_mfma_f32_16x16x32_bf16 v[100:103], v[224:227], v[56:59], v[100:103]\n"
      "v_lshlrev_b32 v35, 16, v122\n"
      "v_and_b32 v34, s58, v122\n"
      "v_mov_b32 v32, v120\n"
      "v_add_f32 v35, v35, v34\n"
      "v_mfma_f32_16x16x32_bf16 v[104:107], v[220:223], v[60:63], v[104:107]\n"
      "v_mov_b32 v40, v120\n"
      "v_rcp_f32 v34, v35\n"
      "v_mov_b32 v33, v122\n"
      "v_mov_b32 v41, v122\n"
      "v_mfma_f32_16x16x32_bf16 v[108:111], v[224:227], v[60:63], v[108:111]\n"
      "v_mul_f32 v28, v34, v34\n"
      "v_mul_f32 v34, 0xc0135761, v34\n"
      "v_mul_f32 v28, 0x3d372713, v28\n"
      "v_mov_b32 v43, v35\n"
      "v_mfma_f32_16x16x32_bf16 v[16:19], v[12:15], v[220:223], v[16:19]\n"
      "v_mul_f32 v28, v28, v34\n"
      "v_mov_b32 v42, v34\n"
      "v_mov_b32 v10, v28\n"
      "v_mfma_f32_16x16x32_bf16 v[20:23], v[12:15], v[224:227], v[20:23]\n"
      "v_permlane32_swap_b32 v32, v40\n"
      "v_permlane32_swap_b32 v33, v41\n"
      "v_permlane32_swap_b32 v35, v43\n"
      "s_waitcnt lgkmcnt(0)\n"
      "v_mfma_f32_16x16x32_bf16 v[80:83], v[228:231], v[64:67], v[80:83]\n"
      "v_mfma_f32_16x16x32_bf16 v[84:87], v[232:235], v[64:67], v[84:87]\n"
      "s_barrier\n"
      "ds_read_b64_tr_b16 v[236:237], v116 offset:24576\n"
      "ds_read_b64_tr_b16 v[238:239], v116 offset:25600\n"
      "v_mfma_f32_16x16x32_bf16 v[88:91], v[228:231], v[68:71], v[88:91]\n"
      "ds_read_b128 v[48:51], v118 offset:24576\n"
      "ds_read_b64_tr_b16 v[240:241], v117 offset:24576\n"
      "v_mfma_f32_16x16x32_bf16 v[92:95], v[232:235], v[68:71], v[92:95]\n"
      "ds_read_b64_tr_b16 v[242:243], v117 offset:25600\n"
      "ds_read_b128 v[52:55], v118 offset:26624\n"
      "v_mfma_f32_16x16x32_bf16 v[96:99], v[228:231], v[72:75], v[96:99]\n"
      "ds_read_b128 v[56:59], v118 offset:28672\n"
      "ds_read_b128 v[60:63], v118 offset:30720\n"
      "v_mfma_f32_16x16x32_bf16 v[100:103], v[232:235], v[72:75], v[100:103]\n"
      "v_permlane32_swap_b32 v34, v42\n"
      "v_permlane32_swap_b32 v28, v10\n"
      "v_mov_b32 v36, v32\n"
      "v_mov_b32 v44, v40\n"
      "v_mov_b32 v37, v33\n"
      "v_mfma_f32_16x16x32_bf16 v[104:107], v[228:231], v[76:79], v[104:107]\n"
      "v_mov_b32 v45, v41\n"
      "v_mov_b32 v39, v35\n"
      "v_mov_b32 v47, v43\n"
      "v_mov_b32 v38, v34\n"
      "v_mov_b32 v46, v42\n"
      "v_mfma_f32_16x16x32_bf16 v[108:111], v[232:235], v[76:79], v[108:111]\n"
      "v_mov_b32 v30, v28\n"
      "v_mov_b32 v252, v10\n"
      "v_permlane16_swap_b32 v32, v36\n"
      "v_permlane16_swap_b32 v40, v44\n"
      "v_mfma_f32_16x16x32_bf16 v[16:19], v[12:15], v[228:231], v[16:19]\n"
      "v_permlane16_swap_b32 v33, v37\n"
      "v_permlane16_swap_b32 v41, v45\n"
      "v_permlane16_swap_b32 v35, v39\n"
      "v_permlane16_swap_b32 v43, v47\n"
      "v_mfma_f32_16x16x32_bf16 v[20:23], v[12:15], v[232:235], v[20:23]\n"
      "v_permlane16_swap_b32 v34, v38\n"
      "v_permlane16_swap_b32 v42, v46\n"
      "v_permlane16_swap_b32 v28, v30\n"
      "v_permlane16_swap_b32 v10, v252\n"
      "s_waitcnt lgkmcnt(0)\n"
      "v_mfma_f32_16x16x32_bf16 v[80:83], v[236:239], v[48:51], v[80:83]\n"
      "ds_read_b64_tr_b16 v[244:245], v116 offset:32768\n"
      "ds_read_b64_tr_b16 v[246:247], v116 offset:33792\n"
      "v_mfma_f32_16x16x32_bf16 v[84:87], v[240:243], v[48:51], v[84:87]\n"
      "ds_read_b128 v[64:67], v119 offset:24576\n"
      "ds_read_b64_tr_b16 v[248:249], v117 offset:32768\n"
      "v_mfma_f32_16x16x32_bf16 v[88:91], v[236:239], v[52:55], v[88:91]\n"
      "ds_read_b64_tr_b16 v[250:251], v117 offset:33792\n"
      "ds_read_b128 v[68:71], v119 offset:26624\n"
      "v_mfma_f32_16x16x32_bf16 v[92:95], v[240:243], v[52:55], v[92:95]\n"
      "ds_read_b128 v[72:75], v119 offset:28672\n"
      "ds_read_b128 v[76:79], v119 offset:30720\n"
      "v_mfma_f32_16x16x32_bf16 v[96:99], v[236:239], v[56:59], v[96:99]\n"
      "v_mfma_f32_16x16x32_bf16 v[100:103], v[240:243], v[56:59], v[100:103]\n"
      "v_mfma_f32_16x16x32_bf16 v[104:107], v[236:239], v[60:63], v[104:107]\n"
      "v_mfma_f32_16x16x32_bf16 v[108:111], v[240:243], v[60:63], v[108:111]\n"
      "v_mfma_f32_16x16x32_bf16 v[16:19], v[12:15], v[236:239], v[16:19]\n"
      "v_mfma_f32_16x16x32_bf16 v[20:23], v[12:15], v[240:243], v[20:23]\n"
      "s_waitcnt lgkmcnt(0)\n"
      "v_mfma_f32_16x16x32_bf16 v[16:19], v[12:15], v[244:247], v[16:19]\n"
      "v_mfma_f32_16x16x32_bf16 v[20:23], v[12:15], v[248:251], v[20:23]\n"
      "v_mfma_f32_16x16x32_bf16 v[80:83], v[244:247], v[64:67], v[80:83]\n"
      "s_nop 5\n"
      "v_cvt_pk_bf16_f32 v17, v16, v16\n"
      "v_cvt_pk_bf16_f32 v21, v20, v20\n"
      "v_mfma_f32_16x16x32_bf16 v[84:87], v[248:251], v[64:67], v[84:87]\n"
      "v_lshlrev_b32 v18, 16, v17\n"
      "v_lshlrev_b32 v22, 16, v21\n"
      "v_sub_f32 v16, v16, v18\n"
      "v_mfma_f32_16x16x32_bf16 v[88:91], v[244:247], v[68:71], v[88:91]\n"
      "v_sub_f32 v20, v20, v22\n"
      "v_cndmask_b32 v24, 0, v17, s[70:71]\n"
      "v_cndmask_b32 v26, 0, v21, s[70:71]\n"
      "v_mfma_f32_16x16x32_bf16 v[92:95], v[248:251], v[68:71], v[92:95]\n"
      "v_cvt_pk_bf16_f32 v17, v16, v16\n"
      "v_cvt_pk_bf16_f32 v21, v20, v20\n"
      "v_lshlrev_b32 v18, 16, v17\n"
      "v_mfma_f32_16x16x32_bf16 v[96:99], v[244:247], v[72:75], v[96:99]\n"
      "v_lshlrev_b32 v22, 16, v21\n"
      "v_sub_f32 v16, v16, v18\n"
      "v_sub_f32 v20, v20, v22\n"
      "v_mfma_f32_16x16x32_bf16 v[100:103], v[248:251], v[72:75], v[100:103]\n"
      "v_cndmask_b32 v24, v24, v17, s[72:73]\n"
      "v_cndmask_b32 v26, v26, v21, s[72:73]\n"
      "v_mfma_f32_16x16x32_bf16 v[104:107], v[244:247], v[76:79], v[104:107]\n"
      "v_cvt_pk_bf16_f32 v17, v16, v16\n"
      "v_cvt_pk_bf16_f32 v21, v20, v20\n"
      "v_mfma_f32_16x16x32_bf16 v[108:111], v[248:251], v[76:79], v[108:111]\n"
      "v_cndmask_b32 v24, v24, v17, s[74:75]\n"
      "v_cndmask_b32 v26, v26, v21, s[74:75]\n"
      "s_nop 0\n"
      "s_branch .Lc_g0\n"
      ".Lc_gretry0:\n"
      "s_sub_u32 s82, s82, 1\n"
      "s_cbranch_scc1 .Lc_gok0\n"
      "s_sleep 6\n"
      "buffer_load_dwordx4 v[120:123], v7, s[20:23], 0 offen offset:0 sc1\n"
      "s_waitcnt vmcnt(0)\n"
      "s_branch .Lc_gchk0\n"
      ".Lc_gretry1:\n"
      "s_sub_u32 s82, s82, 1\n"
      "s_cbranch_scc1 .Lc_gok1\n"
      "s_sleep 6\n"
      "buffer_load_dwordx4 v[120:123], v7, s[20:23], 0 offen offset:1024 sc1\n"
      "s_waitcnt vmcnt(0)\n"
      "s_branch .Lc_gchk1\n"
      ".p2align 12\n"
      ".Lpad_b:\n"
      ".Lpad_b2:\n"
      "s_setpc_b64 s[6:7]\n"
      ".Lc_g0:\n"
      "v_mfma_f32_16x16x16_bf16 v[80:83], v[24:25], v[32:33], v[80:83]\n"
      "v_mfma_f32_16x16x16_bf16 v[84:87], v[26:27], v[32:33], v[84:87]\n"
      "v_mfma_f32_16x16x16_bf16 v[88:91], v[24:25], v[36:37], v[88:91]\n"
      "v_mfma_f32_16x16x16_bf16 v[92:95], v[26:27], v[36:37], v[92:95]\n"
      "v_mfma_f32_16x16x16_bf16 v[96:99], v[24:25], v[40:41], v[96:99]\n"
      "v_mfma_f32_16x16x16_bf16 v[100:103], v[26:27], v[40:41], v[100:103]\n"
      "v_mfma_f32_16x16x16_bf16 v[104:107], v[24:25], v[44:45], v[104:107]\n"
      "v_mfma_f32_16x16x16_bf16 v[108:111], v[26:27], v[44:45], v[108:111]\n"
      "v_mov_b32 v16, v35\n"
      "v_mov_b32 v18, v39\n"
      "v_mov_b32 v20, v43\n"
      "v_mov_b32 v22, v47\n"
      "v_pk_mul_f32 v[64:65], v[80:81], v[80:81]\n"
      "v_pk_mul_f32 v[66:67], v[82:83], v[82:83]\n"
      "v_pk_mul_f32 v[68:69], v[84:85], v[84:85]\n"
      "v_pk_mul_f32 v[70:71], v[86:87], v[86:87]\n"
      "v_pk_mul_f32 v[72:73], v[88:89], v[88:89]\n"
      "v_pk_mul_f32 v[74:75], v[90:91], v[90:91]\n"
      "v_pk_mul_f32 v[76:77], v[92:93], v[92:93]\n"
      "v_pk_mul_f32 v[78:79], v[94:95], v[94:95]\n"
      "v_pk_fma_f32 v[64:65], v[64:65], v[28:29], v[34:35] op_sel_hi:[1,0,0]\n"
      "v_pk_fma_f32 v[66:67], v[66:67], v[28:29], v[34:35] op_sel_hi:[1,0,0]\n"
      "v_pk_fma_f32 v[68:69], v[68:69], v[28:29], v[34:35] op_sel_hi:[1,0,0]\n"
      "v_pk_fma_f32 v[70:71], v[70:71], v[28:29], v[34:35] op_sel_hi:[1,0,0]\n"
      "v_pk_fma_f32 v[72:73], v[72:73], v[30:31], v[38:39] op_sel_hi:[1,0,0]\n"
      "v_pk_fma_f32 v[74:75], v[74:75], v[30:31], v[38:39] op_sel_hi:[1,0,0]\n"
      "v_pk_fma_f32 v[76:77], v[76:77], v[30:31], v[38:39] op_sel_hi:[1,0,0]\n"
      "v_pk_fma_f32 v[78:79], v[78:79], v[30:31], v[38:39] op_sel_hi:[1,0,0]\n"
      "v_pk_mul_f32 v[64:65], v[64:65], v[80:81]\n"
      "v_pk_mul_f32 v[66:67], v[66:67], v[82:83]\n"
      "v_pk_mul_f32 v[68:69], v[68:69], v[84:85]\n"
      "v_pk_mul_f32 v[70:71], v[70:71], v[86:87]\n"
      "v_pk_mul_f32 v[72:73], v[72:73], v[88:89]\n"
      "v_pk_mul_f32 v[74:75], v[74:75], v[90:91]\n"
      "v_pk_mul_f32 v[76:77], v[76:77], v[92:93]\n"
      "v_pk_mul_f32 v[78:79], v[78:79], v[94:95]\n"
      "v_exp_f32 v64, v64\n"
      "v_exp_f32 v65, v65\n"
      "v_exp_f32 v66, v66\n"
      "v_exp_f32 v67, v67\n"
      "v_exp_f32 v68, v68\n"
      "v_exp_f32 v69, v69\n"
      "v_exp_f32 v70, v70\n"
      "v_exp_f32 v71, v71\n"
      "v_exp_f32 v72, v72\n"
      "v_exp_f32 v73, v73\n"
      "v_exp_f32 v74, v74\n"
      "v_exp_f32 v75, v75\n"
      "v_exp_f32 v76, v76\n"
      "v_exp_f32 v77, v77\n"
      "v_exp_f32 v78, v78\n"
      "v_exp_f32 v79, v79\n"
      "v_pk_fma_f32 v[64:65], v[64:65], v[16:17], v[16:17] op_sel_hi:[1,0,0]\n"
      "v_pk_fma_f32 v[66:67], v[66:67], v[16:17], v[16:17] op_sel_hi:[1,0,0]\n"
      "v_pk_fma_f32 v[68:69], v[68:69], v[16:17], v[16:17] op_sel_hi:[1,0,0]\n"
      "v_pk_fma_f32 v[70:71], v[70:71], v[16:17], v[16:17] op_sel_hi:[1,0,0]\n"
      "v_pk_fma_f32 v[72:73], v[72:73], v[18:19], v[18:19] op_sel_hi:[1,0,0]\n"
      "v_pk_fma_f32 v[74:75], v[74:75], v[18:19], v[18:19] op_sel_hi:[1,0,0]\n"
      "v_pk_fma_f32 v[76:77], v[76:77], v[18:19], v[18:19] op_sel_hi:[1,0,0]\n"
      "v_pk_fma_f32 v[78:79], v[78:79], v[18:19], v[18:19] op_sel_hi:[1,0,0]\n"
      "v_rcp_f32 v64, v64\n"
      "v_rcp_f32 v65, v65\n"
      "v_rcp_f32 v66, v66\n"
      "v_rcp_f32 v67, v67\n"
      "v_rcp_f32 v68, v68\n"
      "v_rcp_f32 v69, v69\n"
      "v_rcp_f32 v70, v70\n"
      "v_rcp_f32 v71, v71\n"
      "v_rcp_f32 v72, v72\n"
      "v_rcp_f32 v73, v73\n"
      "v_rcp_f32 v74, v74\n"
      "v_rcp_f32 v75, v75\n"
      "v_rcp_f32 v76, v76\n"
      "v_rcp_f32 v77, v77\n"
      "v_rcp_f32 v78, v78\n"
      "v_rcp_f32 v79, v79\n"
      "v_pk_mul_f32 v[64:65], v[64:65], v[80:81]\n"
      "v_pk_mul_f32 v[66:67], v[66:67], v[82:83]\n"
      "v_pk_mul_f32 v[68:69], v[68:69], v[84:85]\n"
      "v_pk_mul_f32 v[70:71], v[70:71], v[86:87]\n"
      "v_pk_mul_f32 v[72:73], v[72:73], v[88:89]\n"
      "v_pk_mul_f32 v[74:75], v[74:75], v[90:91]\n"
      "v_pk_mul_f32 v[76:77], v[76:77], v[92:93]\n"
      "v_pk_mul_f32 v[78:79], v[78:79], v[94:95]\n"
      "v_cvt_pk_bf16_f32 v80, v64, v65\n"
      "v_cvt_pk_bf16_f32 v81, v66, v67\n"
      "v_cvt_pk_bf16_f32 v82, v68, v69\n"
      "v_cvt_pk_bf16_f32 v83, v70, v71\n"
      "v_cvt_pk_bf16_f32 v84, v72, v73\n"
      "v_cvt_pk_bf16_f32 v85, v74, v75\n"
      "v_cvt_pk_bf16_f32 v86, v76, v77\n"
      "v_cvt_pk_bf16_f32 v87, v78, v79\n"
      "buffer_store_dwordx4 v[80:83], v6, s[20:23], s69 offen sc1\n"
      "buffer_store_dwordx4 v[84:87], v6, s[20:23], s69 offen offset:1024 sc1\n"
      "s_add_u32 s69, s69, 115712\n"
      "v_pk_mul_f32 v[64:65], v[96:97], v[96:97]\n"
      "v_pk_mul_f32 v[66:67], v[98:99], v[98:99]\n"
      "v_pk_mul_f32 v[68:69], v[100:101], v[100:101]\n"
      "v_pk_mul_f32 v[70:71], v[102:103], v[102:103]\n"
      "v_pk_mul_f32 v[72:73], v[104:105], v[104:105]\n"
      "v_pk_mul_f32 v[74:75], v[106:107], v[106:107]\n"
      "v_pk_mul_f32 v[76:77], v[108:109], v[108:109]\n"
      "v_pk_mul_f32 v[78:79], v[110:111], v[110:111]\n"
      "v_pk_fma_f32 v[64:65], v[64:65], v[10:11], v[42:43] op_sel_hi:[1,0,0]\n"
      "v_pk_fma_f32 v[66:67], v[66:67], v[10:11], v[42:43] op_sel_hi:[1,0,0]\n"
      "v_pk_fma_f32 v[68:69], v[68:69], v[10:11], v[42:43] op_sel_hi:[1,0,0]\n"
      "v_pk_fma_f32 v[70:71], v[70:71], v[10:11], v[42:43] op_sel_hi:[1,0,0]\n"
      "v_pk_fma_f32 v[72:73], v[72:73], v[252:253], v[46:47] op_sel_hi:[1,0,0]\n"
      "v_pk_fma_f32 v[74:75], v[74:75], v[252:253], v[46:47] op_sel_hi:[1,0,0]\n"
      "v_pk_fma_f32 v[76:77], v[76:77], v[252:253], v[46:47] op_sel_hi:[1,0,0]\n"
      "v_pk_fma_f32 v[78:79], v[78:79], v[252:253], v[46:47] op_sel_hi:[1,0,0]\n"
      "v_pk_mul_f32 v[64:65], v[64:65], v[96:97]\n"
      "v_pk_mul_f32 v[66:67], v[66:67], v[98:99]\n"
      "v_pk_mul_f32 v[68:69], v[68:69], v[100:101]\n"
      "v_pk_mul_f32 v[70:71], v[70:71], v[102:103]\n"
      "v_pk_mul_f32 v[72:73], v[72:73], v[104:105]\n"
      "v_pk_mul_f32 v[74:75], v[74:75], v[106:107]\n"
      "v_pk_mul_f32 v[76:77], v[76:77], v[108:109]\n"
      "v_pk_mul_f32 v[78:79], v[78:79], v[110:111]\n"
      "v_exp_f32 v64, v64\n"
      "v_exp_f32 v65, v65\n"
      "v_exp_f32 v66, v66\n"
      "v_exp_f32 v67, v67\n"
      "v_exp_f32 v68, v68\n"
      "v_exp_f32 v69, v69\n"
      "v_exp_f32 v70, v70\n"
      "v_exp_f32 v71, v71\n"
      "v_exp_f32 v72, v72\n"
      "v_exp_f32 v73, v73\n"
      "v_exp_f32 v74, v74\n"
      "v_exp_f32 v75, v75\n"
      "v_exp_f32 v76, v76\n"
      "v_exp_f32 v77, v77\n"
      "v_exp_f32 v78, v78\n"
      "v_exp_f32 v79, v79\n"
      "v_pk_fma_f32 v[64:65], v[64:65], v[20:21], v[20:21] op_sel_hi:[1,0,0]\n"
      "v_pk_fma_f32 v[66:67], v[66:67], v[20:21], v[20:21] op_sel_hi:[1,0,0]\n"
      "v_pk_fma_f32 v[68:69], v[68:69], v[20:21], v[20:21] op_sel_hi:[1,0,0]\n"
      "v_pk_fma_f32 v[70:71], v[70:71], v[20:21], v[20:21] op_sel_hi:[1,0,0]\n"
      "v_pk_fma_f32 v[72:73], v[72:73], v[22:23], v[22:23] op_sel_hi:[1,0,0]\n"
      "v_pk_fma_f32 v[74:75], v[74:75], v[22:23], v[22:23] op_sel_hi:[1,0,0]\n"
      "v_pk_fma_f32 v[76:77], v[76:77], v[22:23], v[22:23] op_sel_hi:[1,0,0]\n"
      "v_pk_fma_f32 v[78:79], v[78:79], v[22:23], v[22:23] op_sel_hi:[1,0,0]\n"
      "v_rcp_f32 v64, v64\n"
      "v_rcp_f32 v65, v65\n"
      "v_rcp_f32 v66, v66\n"
      "v_rcp_f32 v67, v67\n"
      "v_rcp_f32 v68, v68\n"
      "v_rcp_f32 v69, v69\n"
      "v_rcp_f32 v70, v70\n"
      "v_rcp_f32 v71, v71\n"
      "v_rcp_f32 v72, v72\n"
      "v_rcp_f32 v73, v73\n"
      "v_rcp_f32 v74, v74\n"
      "v_rcp_f32 v75, v75\n"
      "v_rcp_f32 v76, v76\n"
      "v_rcp_f32 v77, v77\n"
      "v_rcp_f32 v78, v78\n"
      "v_rcp_f32 v79, v79\n"
      "v_pk_mul_f32 v[64:65], v[64:65], v[96:97]\n"
      "v_pk_mul_f32 v[66:67], v[66:67], v[98:99]\n"
      "v_pk_mul_f32 v[68:69], v[68:69], v[100:101]\n"
      "v_pk_mul_f32 v[70:71], v[70:71], v[102:103]\n"
      "v_pk_mul_f32 v[72:73], v[72:73], v[104:105]\n"
      "v_pk_mul_f32 v[74:75], v[74:75], v[106:107]\n"
      "v_pk_mul_f32 v[76:77], v[76:77], v[108:109]\n"
      "v_pk_mul_f32 v[78:79], v[78:79], v[110:111]\n"
      "v_cvt_pk_bf16_f32 v96, v64, v65\n"
      "v_cvt_pk_bf16_f32 v97, v66, v67\n"
      "v_cvt_pk_bf16_f32 v98, v68, v69\n"
      "v_cvt_pk_bf16_f32 v99, v70, v71\n"
      "v_cvt_pk_bf16_f32 v100, v72, v73\n"
      "v_cvt_pk_bf16_f32 v101, v74, v75\n"
      "v_cvt_pk_bf16_f32 v102, v76, v77\n"
      "v_cvt_pk_bf16_f32 v103, v78, v79\n"
      "buffer_store_dwordx4 v[96:99], v6, s[20:23], s69 offen sc1\n"
      "buffer_store_dwordx4 v[100:103], v6, s[20:23], s69 offen offset:1024 sc1\n"
      "s_add_u32 s69, s69, 115712\n"
      "s_setpc_b64 s[64:65]\n"
      ".Lc_b:\n"
      "v_add_u32 v4, 81920, v4\n"
      "v_add_u32 v5, 81920, v5\n"
      "v_mov_b32 v13, s60\n"
      "v_not_b32 v12, v13\n"
      "s_barrier\n"
      "ds_read_b128 v[48:51], v4 offset:0\n"
      "ds_read_b128 v[52:55], v4 offset:2048\n"
      "ds_read_b128 v[56:59], v4 offset:4096\n"
      "ds_read_b128 v[60:63], v4 offset:6144\n"
      "buffer_load_dwordx4 v[120:123], v7, s[20:23], 0 offen offset:1024 sc1\n"
      "s_waitcnt lgkmcnt(3)\n"
      "v_mfma_f32_16x16x32_bf16 v[80:83], v[124:127], v[48:51], 0\n"
      "ds_read_b128 v[64:67], v5 offset:0\n"
      "v_mfma_f32_16x16x32_bf16 v[84:87], v[128:131], v[48:51], 0\n"
      "ds_read_b128 v[68:71], v5 offset:2048\n"
      "s_waitcnt lgkmcnt(4)\n"
      "v_mfma_f32_16x16x32_bf16 v[88:91], v[124:127], v[52:55], 0\n"
      "ds_read_b128 v[72:75], v5 offset:4096\n"
      "v_mfma_f32_16x16x32_bf16 v[92:95], v[128:131], v[52:55], 0\n"
      "ds_read_b128 v[76:79], v5 offset:6144\n"
      "s_waitcnt lgkmcnt(5)\n"
      "v_mfma_f32_16x16x32_bf16 v[96:99], v[124:127], v[56:59], 0\n"
      "v_mfma_f32_16x16x32_bf16 v[100:103], v[128:131], v[56:59], 0\n"
      "s_waitcnt lgkmcnt(4)\n"
      "v_mfma_f32_16x16x32_bf16 v[104:107], v[124:127], v[60:63], 0\n"
      "v_mfma_f32_16x16x32_bf16 v[108:111], v[128:131], v[60:63], 0\n"
      "s_waitcnt lgkmcnt(3)\n"
      "v_mfma_f32_16x16x32_bf16 v[80:83], v[132:135], v[64:67], v[80:83]\n"
      "ds_read_b128 v[48:51], v4 offset:8192\n"
      "v_mfma_f32_16x16x32_bf16 v[84:87], v[136:139], v[64:67], v[84:87]\n"
      "ds_read_b128 v[52:55], v4 offset:10240\n"
      "s_waitcnt lgkmcnt(4)\n"
      "v_mfma_f32_16x16x32_bf16 v[88:91], v[132:135], v[68:71], v[88:91]\n"
      "ds_read_b128 v[56:59], v4 offset:12288\n"
      "v_mfma_f32_16x16x32_bf16 v[92:95], v[136:139], v[68:71], v[92:95]\n"
      "ds_read_b128 v[60:63], v4 offset:14336\n"
      "s_waitcnt lgkmcnt(5)\n"
      "v_mfma_f32_16x16x32_bf16 v[96:99], v[132:135], v[72:75], v[96:99]\n"
      "v_mfma_f32_16x16x32_bf16 v[100:103], v[136:139], v[72:75], v[100:103]\n"
      "s_waitcnt lgkmcnt(4)\n"
      "v_mfma_f32_16x16x32_bf16 v[104:107], v[132:135], v[76:79], v[104:107]\n"
      "v_mfma_f32_16x16x32_bf16 v[108:111], v[136:139], v[76:79], v[108:111]\n"
      "s_waitcnt lgkmcnt(3)\n"
      "v_mfma_f32_16x16x32_bf16 v[80:83], v[140:143], v[48:51], v[80:83]\n"
      "ds_read_b128 v[64:67], v5 offset:8192\n"
      "v_mfma_f32_16x16x32_bf16 v[84:87], v[144:147], v[48:51], v[84:87]\n"
      "ds_read_b128 v[68:71], v5 offset:10240\n"
      "s_waitcnt lgkmcnt(4)\n"
      "v_mfma_f32_16x16x32_bf16 v[88:91], v[140:143], v[52:55], v[88:91]\n"
      "ds_read_b128 v[72:75], v5 offset:12288\n"
      "v_mfma_f32_16x16x32_bf16 v[92:95], v[144:147], v[52:55], v[92:95]\n"
      "ds_read_b128 v[76:79], v5 offset:14336\n"
      "s_waitcnt lgkmcnt(5)\n"
      "v_mfma_f32_16x16x32_bf16 v[96:99], v[140:143], v[56:59], v[96:99]\n"
      "v_mfma_f32_16x16x32_bf16 v[100:103], v[144:147], v[56:59], v[100:103]\n"
      "s_waitcnt lgkmcnt(4)\n"
      "v_mfma_f32_16x16x32_bf16 v[104:107], v[140:143], v[60:63], v[104:107]\n"
      "v_mfma_f32_16x16x32_bf16 v[108:111], v[144:147], v[60:63], v[108:111]\n"
      "s_waitcnt lgkmcnt(3)\n"
      "v_mfma_f32_16x16x32_bf16 v[80:83], v[148:151], v[64:67], v[80:83]\n"
      "ds_read_b128 v[48:51], v4 offset:16384\n"
      "v_mfma_f32_16x16x32_bf16 v[84:87], v[152:155], v[64:67], v[84:87]\n"
      "ds_read_b128 v[52:55], v4 offset:18432\n"
      "s_waitcnt lgkmcnt(4)\n"
      "v_mfma_f32_16x16x32_bf16 v[88:91], v[148:151], v[68:71], v[88:91]\n"
      "ds_read_b128 v[56:59], v4 offset:20480\n"
      "v_mfma_f32_16x16x32_bf16 v[92:95], v[152:155], v[68:71], v[92:95]\n"
      "ds_read_b128 v[60:63], v4 offset:22528\n"
      "s_waitcnt lgkmcnt(5)\n"
      "v_mfma_f32_16x16x32_bf16 v[96:99], v[148:151], v[72:75], v[96:99]\n"
      "v_mfma_f32_16x16x32_bf16 v[100:103], v[152:155], v[72:75], v[100:103]\n"
      "s_waitcnt lgkmcnt(4)\n"
      "v_mfma_f32_16x16x32_bf16 v[104:107], v[148:151], v[76:79], v[104:107]\n"
      "v_mfma_f32_16x16x32_bf16 v[108:111], v[152:155], v[76:79], v[108:111]\n"
      "s_waitcnt lgkmcnt(3)\n"
      "v_mfma_f32_16x16x32_bf16 v[80:83], v[156:159], v[48:51], v[80:83]\n"
      "ds_read_b128 v[64:67], v5 offset:16384\n"
      "v_mfma_f32_16x16x32_bf16 v[84:87], v[160:163], v[48:51], v[84:87]\n"
      "ds_read_b128 v[68:71], v5 offset:18432\n"
      "s_waitcnt lgkmcnt(4)\n"
      "v_mfma_f32_16x16x32_bf16 v[88:91], v[156:159], v[52:55], v[88:91]\n"
      "ds_read_b128 v[72:75], v5 offset:20480\n"
      "v_mfma_f32_16x16x32_bf16 v[92:95], v[160:163], v[52:55], v[92:95]\n"
      "ds_read_b128 v[76:79], v5 offset:22528\n"
      "s_waitcnt lgkmcnt(5)\n"
      "v_mfma_f32_16x16x32_bf16 v[96:99], v[156:159], v[56:59], v[96:99]\n"
      "v_mfma_f32_16x16x32_bf16 v[100:103], v[160:163], v[56:59], v[100:103]\n"
      "s_waitcnt lgkmcnt(4)\n"
      "v_mfma_f32_16x16x32_bf16 v[104:107], v[156:159], v[60:63], v[104:107]\n"
      "v_mfma_f32_16x16x32_bf16 v[108:111], v[160:163], v[60:63], v[108:111]\n"
      "s_waitcnt lgkmcnt(3)\n"
      "v_mfma_f32_16x16x32_bf16 v[80:83], v[164:167], v[64:67], v[80:83]\n"
      "ds_read_b128 v[48:51], v4 offset:24576\n"
      "v_mfma_f32_16x16x32_bf16 v[84:87], v[168:171], v[64:67], v[84:87]\n"
      "ds_read_b128 v[52:55], v4 offset:26624\n"
      "s_waitcnt lgkmcnt(4)\n"
      "v_mfma_f32_16x16x32_bf16 v[88:91], v[164:167], v[68:71], v[88:91]\n"
      "ds_read_b128 v[56:59], v4 offset:28672\n"
      "v_mfma_f32_16x16x32_bf16 v[92:95], v[168:171], v[68:71], v[92:95]\n"
      "ds_read_b128 v[60:63], v4 offset:30720\n"
      "s_waitcnt lgkmcnt(5)\n"
      "v_mfma_f32_16x16x32_bf16 v[96:99], v[164:167], v[72:75], v[96:99]\n"
      "v_mfma_f32_16x16x32_bf16 v[100:103], v[168:171], v[72:75], v[100:103]\n"
      "s_waitcnt lgkmcnt(4)\n"
      "v_mfma_f32_16x16x32_bf16 v[104:107], v[164:167], v[76:79], v[104:107]\n"
      "v_mfma_f32_16x16x32_bf16 v[108:111], v[168:171], v[76:79], v[108:111]\n"
      "s_waitcnt vmcnt(0)\n"
      "s_mov_b64 exec, 1\n"
      "buffer_store_dwordx2 v[12:13], off, s[20:23], s85 offset:1024 sc1\n"
      "s_mov_b64 exec, -1\n"
      ".Lc_gchk1:\n"
      "v_cmp_ne_u32 s[76:77], s60, v121\n"
      "v_cmp_ne_u32 vcc, s60, v123\n"
      "s_or_b64 vcc, vcc, s[76:77]\n"
      "s_cbranch_scc1 .Lc_gretry1\n"
      ".Lc_gok1:\n"
      "s_waitcnt lgkmcnt(3)\n"
      "v_mfma_f32_16x16x32_bf16 v[80:83], v[172:175], v[48:51], v[80:83]\n"
      "ds_read_b128 v[64:67], v5 offset:24576\n"
      "v_mfma_f32_16x16x32_bf16 v[84:87], v[176:179], v[48:51], v[84:87]\n"
      "ds_read_b128 v[68:71], v5 offset:26624\n"
      "s_waitcnt lgkmcnt(4)\n"
      "v_mfma_f32_16x16x32_bf16 v[88:91], v[172:175], v[52:55], v[88:91]\n"
      "ds_read_b128 v[72:75], v5 offset:28672\n"
      "v_mfma_f32_16x16x32_bf16 v[92:95], v[176:179], v[52:55], v[92:95]\n"
      "ds_read_b128 v[76:79], v5 offset:30720\n"
      "s_waitcnt lgkmcnt(5)\n"
      "v_mfma_f32_16x16x32_bf16 v[96:99], v[172:175], v[56:59], v[96:99]\n"
      "v_lshlrev_b32 v35, 16, v122\n"
      "v_and_b32 v34, s58, v122\n"
      "v_mfma_f32_16x16x32_bf16 v[100:103], v[176:179], v[56:59], v[100:103]\n"
      "v_mov_b32 v32, v120\n"
      "s_waitcnt lgkmcnt(4)\n"
      "v_mfma_f32_16x16x32_bf16 v[104:107], v[172:175], v[60:63], v[104:107]\n"
      "v_add_f32 v35, v35, v34\n"
      "v_mfma_f32_16x16x32_bf16 v[108:111], v[176:179], v[60:63], v[108:111]\n"
      "v_mov_b32 v40, v120\n"
      "s_waitcnt lgkmcnt(3)\n"
      "v_mfma_f32_16x16x32_bf16 v[80:83], v[180:183], v[64:67], v[80:83]\n"
      "ds_read_b128 v[48:51], v4 offset:32768\n"
      "v_mfma_f32_16x16x32_bf16 v[84:87], v[184:187], v[64:67], v[84:87]\n"
      "ds_read_b128 v[52:55], v4 offset:34816\n"
      "s_waitcnt lgkmcnt(4)\n"
      "v_mfma_f32_16x16x32_bf16 v[88:91], v[180:183], v[68:71], v[88:91]\n"
      "ds_read_b128 v[56:59], v4 offset:36864\n"
      "v_mfma_f32_16x16x32_bf16 v[92:95], v[184:187], v[68:71], v[92:95]\n"
      "ds_read_b128 v[60:63], v4 offset:38912\n"
      "s_waitcnt lgkmcnt(5)\n"
      "v_mfma_f32_16x16x32_bf16 v[96:99], v[180:183], v[72:75], v[96:99]\n"
      "v_rcp_f32 v34, v35\n"
      "v_mov_b32 v33, v122\n"
      "v_mfma_f32_16x16x32_bf16 v[100:103], v[184:187], v[72:75], v[100:103]\n"
      "v_mov_b32 v41, v122\n"
      "s_waitcnt lgkmcnt(4)\n"
      "v_mfma_f32_16x16x32_bf16 v[104:107], v[180:183], v[76:79], v[104:107]\n"
      "v_mul_f32 v28, v34, v34\n"
      "v_mfma_f32_16x16x32_bf16 v[108:111], v[184:187], v[76:79], v[108:111]\n"
      "v_mul_f32 v34, 0xc0135761, v34\n"
      "s_waitcnt lgkmcnt(3)\n"
      "v_mfma_f32_16x16x32_bf16 v[80:83], v[188:191], v[48:51], v[80:83]\n"
      "ds_read_b128 v[64:67], v5 offset:32768\n"
      "v_mfma_f32_16x16x32_bf16 v[84:87], v[192:195], v[48:51], v[84:87]\n"
      "ds_read_b128 v[68:71], v5 offset:34816\n"
      "s_waitcnt lgkmcnt(4)\n"
      "v_mfma_f32_16x16x32_bf16 v[88:91], v[188:191], v[52:55], v[88:91]\n"
      "ds_read_b128 v[72:75], v5 offset:36864\n"
      "v_mfma_f32_16x16x32_bf16 v[92:95], v[192:195], v[52:55], v[92:95]\n"
      "ds_read_b128 v[76:79], v5 offset:38912\n"
      "s_waitcnt lgkmcnt(5)\n"
      "v_mfma_f32_16x16x32_bf16 v[96:99], v[188:191], v[56:59], v[96:99]\n"
      "v_mul_f32 v28, 0x3d372713, v28\n"
      "v_mov_b32 v43, v35\n"
      "v_mfma_f32_16x16x32_bf16 v[100:103], v[192:195], v[56:59], v[100:103]\n"
      "v_mul_f32 v28, v28, v34\n"
      "s_waitcnt lgkmcnt(4)\n"
      "v_mfma_f32_16x16x32_bf16 v[104:107], v[188:191], v[60:63], v[104:107]\n"
      "v_mov_b32 v42, v34\n"
      "v_mfma_f32_16x16x32_bf16 v[108:111], v[192:195], v[60:63], v[108:111]\n"
      "v_mov_b32 v10, v28\n"
      "s_waitcnt lgkmcnt(3)\n"
      "v_mfma_f32_16x16x32_bf16 v[80:83], v[196:199], v[64:67], v[80:83]\n"
      "ds_read_b128 v[48:51], v4 offset:40960\n"
      "v_mfma_f32_16x16x32_bf16 v[84:87], v[200:203], v[64:67], v[84:87]\n"
      "ds_read_b128 v[52:55], v4 offset:43008\n"
      "s_waitcnt lgkmcnt(4)\n"
      "v_mfma_f32_16x16x32_bf16 v[88:91], v[196:199], v[68:71], v[88:91]\n"
      "ds_read_b128 v[56:59], v4 offset:45056\n"
      "v_mfma_f32_16x16x32_bf16 v[92:95], v[200:203], v[68:71], v[92:95]\n"
      "ds_read_b128 v[60:63], v4 offset:47104\n"
      "s_waitcnt lgkmcnt(5)\n"
      "v_mfma_f32_16x16x32_bf16 v[96:99], v[196:199], v[72:75], v[96:99]\n"
      "v_permlane32_swap_b32 v32, v40\n"
      "v_permlane32_swap_b32 v33, v41\n"
      "v_mfma_f32_16x16x32_bf16 v[100:103], v[200:203], v[72:75], v[100:103]\n"
      "v_permlane32_swap_b32 v35, v43\n"
      "s_waitcnt lgkmcnt(4)\n"
      "v_mfma_f32_16x16x32_bf16 v[104:107], v[196:199], v[76:79], v[104:107]\n"
      "v_permlane32_swap_b32 v34, v42\n"
      "v_mfma_f32_16x16x32_bf16 v[108:111], v[200:203], v[76:79], v[108:111]\n"
      "v_permlane32_swap_b32 v28, v10\n"
      "s_waitcnt lgkmcnt(3)\n"
      "v_mfma_f32_16x16x32_bf16 v[80:83], v[204:207], v[48:51], v[80:83]\n"
      "ds_read_b128 v[64:67], v5 offset:40960\n"
      "v_mfma_f32_16x16x32_bf16 v[84:87], v[208:211], v[48:51], v[84:87]\n"
      "ds_read_b128 v[68:71], v5 offset:43008\n"
      "s_waitcnt lgkmcnt(4)\n"
      "v_mfma_f32_16x16x32_bf16 v[88:91], v[204:207], v[52:55], v[88:91]\n"
      "ds_read_b128 v[72:75], v5 offset:45056\n"
      "v_mfma_f32_16x16x32_bf16 v[92:95], v[208:211], v[52:55], v[92:95]\n"
      "ds_read_b128 v[76:79], v5 offset:47104\n"
      "s_waitcnt lgkmcnt(5)\n"
      "v_mfma_f32_16x16x32_bf16 v[96:99], v[204:207], v[56:59], v[96:99]\n"
      "v_mov_b32 v36, v32\n"
      "v_mfma_f32_16x16x32_bf16 v[100:103], v[208:211], v[56:59], v[100:103]\n"
      "v_mov_b32 v44, v40\n"
      "s_waitcnt lgkmcnt(4)\n"
      "v_mfma_f32_16x16x32_bf16 v[104:107], v[204:207], v[60:63], v[104:107]\n"
      "v_mov_b32 v37, v33\n"
      "v_mfma_f32_16x16x32_bf16 v[108:111], v[208:211], v[60:63], v[108:111]\n"
      "v_mov_b32 v45, v41\n"
      "s_waitcnt lgkmcnt(3)\n"
      "v_mfma_f32_16x16x32_bf16 v[80:83], v[212:215], v[64:67], v[80:83]\n"
      "ds_read_b128 v[48:51], v4 offset:49152\n"
      "v_mfma_f32_16x16x32_bf16 v[84:87], v[216:219], v[64:67], v[84:87]\n"
      "ds_read_b128 v[52:55], v4 offset:51200\n"
      "s_waitcnt lgkmcnt(4)\n"
      "v_mfma_f32_16x16x32_bf16 v[88:91], v[212:215], v[68:71], v[88:91]\n"
      "ds_read_b128 v[56:59], v4 offset:53248\n"
      "v_mfma_f32_16x16x32_bf16 v[92:95], v[216:219], v[68:71], v[92:95]\n"
      "ds_read_b128 v[60:63], v4 offset:55296\n"
      "s_waitcnt lgkmcnt(5)\n"
      "v_mfma_f32_16x16x32_bf16 v[96:99], v[212:215], v[72:75], v[96:99]\n"
      "v_mov_b32 v39, v35\n"
      "v_mfma_f32_16x16x32_bf16 v[100:103], v[216:219], v[72:75], v[100:103]\n"
      "v_mov_b32 v47, v43\n"
      "s_waitcnt lgkmcnt(4)\n"
      "v_mfma_f32_16x16x32_bf16 v[104:107], v[212:215], v[76:79], v[104:107]\n"
      "v_mov_b32 v38, v34\n"
      "v_mfma_f32_16x16x32_bf16 v[108:111], v[216:219], v[76:79], v[108:111]\n"
      "v_mov_b32 v46, v42\n"
      "s_waitcnt lgkmcnt(3)\n"
      "v_mfma_f32_16x16x32_bf16 v[80:83], v[220:223], v[48:51], v[80:83]\n"
      "ds_read_b128 v[64:67], v5 offset:49152\n"
      "v_mfma_f32_16x16x32_bf16 v[84:87], v[224:227], v[48:51], v[84:87]\n"
      "ds_read_b128 v[68:71], v5 offset:51200\n"
      "s_waitcnt lgkmcnt(4)\n"
      "v_mfma_f32_16x16x32_bf16 v[88:91], v[220:223], v[52:55], v[88:91]\n"
      "ds_read_b128 v[72:75], v5 offset:53248\n"
      "v_mfma_f32_16x16x32_bf16 v[92:95], v[224:227], v[52:55], v[92:95]\n"
      "ds_read_b128 v[76:79], v5 offset:55296\n"
      "s_waitcnt lgkmcnt(5)\n"
      "v_mfma_f32_16x16x32_bf16 v[96:99], v[220:223], v[56:59], v[96:99]\n"
      "v_mov_b32 v30, v28\n"
      "v_mfma_f32_16x16x32_bf16 v[100:103], v[224:227], v[56:59], v[100:103]\n"
      "v_mov_b32 v252, v10\n"
      "s_waitcnt lgkmcnt(4)\n"
      "v_mfma_f32_16x16x32_bf16 v[104:107], v[220:223], v[60:63], v[104:107]\n"
      "v_permlane16_swap_b32 v32, v36\n"
      "v_mfma_f32_16x16x32_bf16 v[108:111], v[224:227], v[60:63], v[108:111]\n"
      "v_permlane16_swap_b32 v40, v44\n"
      "s_waitcnt lgkmcnt(3)\n"
      "v_mfma_f32_16x16x32_bf16 v[80:83], v[228:231], v[64:67], v[80:83]\n"
      "ds_read_b128 v[48:51], v4 offset:57344\n"
      "v_mfma_f32_16x16x32_bf16 v[84:87], v[232:235], v[64:67], v[84:87]\n"
      "ds_read_b128 v[52:55], v4 offset:59392\n"
      "s_waitcnt lgkmcnt(4)\n"
      "v_mfma_f32_16x16x32_bf16 v[88:91], v[228:231], v[68:71], v[88:91]\n"
      "ds_read_b128 v[56:59], v4 offset:61440\n"
      "v_mfma_f32_16x16x32_bf16 v[92:95], v[232:235], v[68:71], v[92:95]\n"
      "ds_read_b128 v[60:63], v4 offset:63488\n"
      "s_waitcnt lgkmcnt(5)\n"
      "v_mfma_f32_16x16x32_bf16 v[96:99], v[228:231], v[72:75], v[96:99]\n"
      "v_permlane16_swap_b32 v33, v37\n"
      "v_mfma_f32_16x16x32_bf16 v[100:103], v[232:235], v[72:75], v[100:103]\n"
      "v_permlane16_swap_b32 v41, v45\n"
      "s_waitcnt lgkmcnt(4)\n"
      "v_mfma_f32_16x16x32_bf16 v[104:107], v[228:231], v[76:79], v[104:107]\n"
      "v_permlane16_swap_b32 v35, v39\n"
      "v_mfma_f32_16x16x32_bf16 v[108:111], v[232:235], v[76:79], v[108:111]\n"
      "v_permlane16_swap_b32 v43, v47\n"
      "s_waitcnt lgkmcnt(3)\n"
      "v_mfma_f32_16x16x32_bf16 v[80:83], v[236:239], v[48:51], v[80:83]\n"
      "ds_read_b128 v[64:67], v5 offset:57344\n"
      "v_mfma_f32_16x16x32_bf16 v[84:87], v[240:243], v[48:51], v[84:87]\n"
      "ds_read_b128 v[68:71], v5 offset:59392\n"
      "s_waitcnt lgkmcnt(4)\n"
      "v_mfma_f32_16x16x32_bf16 v[88:91], v[236:239], v[52:55], v[88:91]\n"
      "ds_read_b128 v[72:75], v5 offset:61440\n"
      "v_mfma_f32_16x16x32_bf16 v[92:95], v[240:243], v[52:55], v[92:95]\n"
      "ds_read_b128 v[76:79], v5 offset:63488\n"
      "s_waitcnt lgkmcnt(5)\n"
      "v_mfma_f32_16x16x32_bf16 v[96:99], v[236:239], v[56:59], v[96:99]\n"
      "v_permlane16_swap_b32 v34, v38\n"
      "v_mfma_f32_16x16x32_bf16 v[100:103], v[240:243], v[56:59], v[100:103]\n"
      "v_permlane16_swap_b32 v42, v46\n"
      "s_waitcnt lgkmcnt(4)\n"
      "v_mfma_f32_16x16x32_bf16 v[104:107], v[236:239], v[60:63], v[104:107]\n"
      "v_permlane16_swap_b32 v28, v30\n"
      "v_mfma_f32_16x16x32_bf16 v[108:111], v[240:243], v[60:63], v[108:111]\n"
      "v_permlane16_swap_b32 v10, v252\n"
      "s_waitcnt lgkmcnt(3)\n"
      "v_mfma_f32_16x16x32_bf16 v[80:83], v[244:247], v[64:67], v[80:83]\n"
      "v_mfma_f32_16x16x32_bf16 v[84:87], v[248:251], v[64:67], v[84:87]\n"
      "s_waitcnt lgkmcnt(2)\n"
      "v_mfma_f32_16x16x32_bf16 v[88:91], v[244:247], v[68:71], v[88:91]\n"
      "v_mfma_f32_16x16x32_bf16 v[92:95], v[248:251], v[68:71], v[92:95]\n"
      "s_waitcnt lgkmcnt(1)\n"
      "v_mfma_f32_16x16x32_bf16 v[96:99], v[244:247], v[72:75], v[96:99]\n"
      "v_mfma_f32_16x16x32_bf16 v[100:103], v[248:251], v[72:75], v[100:103]\n"
      "s_waitcnt lgkmcnt(0)\n"
      "v_mfma_f32_16x16x32_bf16 v[104:107], v[244:247], v[76:79], v[104:107]\n"
      "v_mfma_f32_16x16x32_bf16 v[108:111], v[248:251], v[76:79], v[108:111]\n"
      "s_swappc_b64 s[64:65], s[66:67]\n"
      "s_waitcnt vmcnt(0)\n"
      "s_mov_b64 exec, 1\n"
      "buffer_store_dwordx2 v[12:13], off, s[20:23], s85 offset:1280 sc1\n"
      "s_mov_b64 exec, -1\n"
      ".Lc_fin:\n"
      "s_mov_b64 s[34:35], s[10:11]\n"
      "v_lshrrev_b32 v32, 3, v1\n"
      "s_lshl_b32 s50, s36, 3\n"
      "v_add_u32 v32, s50, v32\n"
      "v_and_b32 v33, 3, v1\n"
      "v_lshlrev_b32 v33, 2, v33\n"
      "v_xor_b32 v33, v33, v32\n"
      "v_and_b32 v33, 15, v33\n"
      "v_lshlrev_b32 v33, 4, v33\n"
      "v_and_b32 v34, 7, v1\n"
      "v_lshl_add_u32 v2, v34, 9, v33\n"
      "s_lshr_b32 s50, s36, 1\n"
      "s_lshl_b32 s50, s50, 12\n"
      "s_add_u32 s50, s50, 131072\n"
      "v_add_u32 v2, s50, v2\n"
      "v_lshlrev_b32 v34, 4, v34\n"
      "v_lshl_add_u32 v3, v32, 10, v34\n"
      "s_lshl2_add_u32 s50, s45, s46\n"
      "s_lshl_b32 s50, s50, 15\n"
      "s_add_u32 s50, s50, s56\n"
      "s_not_b32 s60, s61\n"
      "s_add_u32 s87, s87, 768\n"
      "v_mov_b32 v11, s61\n"
      "s_or_b32 s54, s54, s36\n"
      "s_mov_b32 s82, 0\n"
      "s_cmp_eq_u32 s48, 1\n"
      "s_cbranch_scc1 .Lc_notidle\n"
      "s_barrier\n"
      "s_sleep 8\n"
      ".Lc_notidle:\n"
      "s_sleep 28\n"
      "s_mov_b32 s52, 65536\n"
      ".Lc_poll:\n"
      "v_min_u32 v40, 13, v1\n"
      "v_lshlrev_b32 v40, 3, v40\n"
      "buffer_load_dwordx2 v[40:41], v40, s[20:23], s87 offen offset:512 sc1\n"
      "s_waitcnt vmcnt(0)\n"
      ".Lc_poll_chk:\n"
      "v_cmp_ne_u64 vcc, s[60:61], v[40:41]\n"
      "s_cbranch_vccz .Lc_poll_ok\n"
      "s_sleep 14\n"
      "s_sub_u32 s52, s52, 1\n"
      "s_cbranch_scc0 .Lc_poll\n"
      ".Lc_poll_ok:\n"
      "s_barrier\n"
      "v_lshlrev_b32 v16, 16, v112\n"
      "v_and_b32 v17, s58, v112\n"
      "v_lshlrev_b32 v18, 16, v113\n"
      "v_and_b32 v19, s58, v113\n"
      "v_lshlrev_b32 v20, 16, v114\n"
      "v_and_b32 v21, s58, v114\n"
      "v_lshlrev_b32 v22, 16, v115\n"
      "v_and_b32 v23, s58, v115\n"
      ".Lc_red:\n"
      "s_barrier\n"
      "ds_read_b128 v[48:51], v2 offset:0\n"
      "ds_read_b128 v[52:55], v2 offset:256\n"
      "ds_read_b128 v[56:59], v2 offset:8192\n"
      "ds_read_b128 v[60:63], v2 offset:8448\n"
      "ds_read_b128 v[64:67], v2 offset:16384\n"
      "ds_read_b128 v[68:71], v2 offset:16640\n"
      "ds_read_b128 v[72:75], v2 offset:24576\n"
      "ds_read_b128 v[76:79], v2 offset:24832\n"
      "s_cmp_lg_u32 s54, 0\n"
      "s_cbranch_scc1 .Lc_nobump\n"
      "s_mov_b64 exec, 1\n"
      "buffer_store_dword v11, off, s[20:23], s88 sc1\n"
      "s_mov_b64 exec, -1\n"
      "s_mov_b32 s54, 1\n"
      ".Lc_nobump:\n"
      "v_subrev_u32 v2, 131072, v2\n"
      "s_waitcnt lgkmcnt(6)\n"
      "v_pk_add_f32 v[48:49], v[48:49], v[16:17]\n"
      "v_pk_add_f32 v[50:51], v[50:51], v[18:19]\n"
      "v_pk_add_f32 v[52:53], v[52:53], v[20:21]\n"
      "v_pk_add_f32 v[54:55], v[54:55], v[22:23]\n"
      "s_waitcnt lgkmcnt(4)\n"
      "v_pk_add_f32 v[48:49], v[48:49], v[56:57]\n"
      "v_pk_add_f32 v[50:51], v[50:51], v[58:59]\n"
      "v_pk_add_f32 v[52:53], v[52:53], v[60:61]\n"
      "v_pk_add_f32 v[54:55], v[54:55], v[62:63]\n"
      "s_waitcnt lgkmcnt(2)\n"
      "v_pk_add_f32 v[48:49], v[48:49], v[64:65]\n"
      "v_pk_add_f32 v[50:51], v[50:51], v[66:67]\n"
      "v_pk_add_f32 v[52:53], v[52:53], v[68:69]\n"
      "v_pk_add_f32 v[54:55], v[54:55], v[70:71]\n"
      "s_waitcnt lgkmcnt(0)\n"
      "v_pk_add_f32 v[48:49], v[48:49], v[72:73]\n"
      "v_pk_add_f32 v[50:51], v[50:51], v[74:75]\n"
      "v_pk_add_f32 v[52:53], v[52:53], v[76:77]\n"
      "v_pk_add_f32 v[54:55], v[54:55], v[78:79]\n"
      "v_cvt_pk_bf16_f32 v32, v48, v49\n"
      "v_cvt_pk_bf16_f32 v33, v50, v51\n"
      "v_cvt_pk_bf16_f32 v34, v52, v53\n"
      "v_cvt_pk_bf16_f32 v35, v54, v55\n"
      "buffer_store_dwordx4 v[32:35], v3, s[32:35], s50 offen sc1\n"
      "s_add_u32 s50, s50, 65536\n"
      "s_add_u32 s82, s82, 1\n"
      "s_cmp_lt_u32 s82, 2\n"
      "s_cbranch_scc1 .Lc_red\n"
      "s_endpgm\n"
      ".p2align 12\n"
      ".Lpad_p:\n"
      ".Lpad_p2:\n"
      "s_setpc_b64 s[6:7]\n"
      ".Ll_p2:\n"
      "s_mov_b32 s52, 65536\n"
      "s_waitcnt vmcnt(4)\n"
      "s_branch .Ll_poll_chk\n"
      ".Ll_poll:\n"
      "v_min_u32 v238, 13, v1\n"
      "v_lshlrev_b32 v238, 3, v238\n"
      "buffer_load_dwordx2 v[238:239], v238, s[20:23], s87 offen offset:512 sc1\n"
      "s_waitcnt vmcnt(0)\n"
      ".Ll_poll_chk:\n"
      "v_cmp_ne_u64 vcc, s[60:61], v[238:239]\n"
      "s_cbranch_vccz .Ll_poll_ok\n"
      "s_sleep 14\n"
      "s_sub_u32 s52, s52, 1\n"
      "s_cbranch_scc0 .Ll_poll\n"
      ".Ll_poll_ok:\n"
      "s_setprio 0\n"
      "buffer_load_dwordx4 v[136:139], v6, s[20:23], s70 offen offset:0 sc1\n"
      "buffer_load_dwordx4 v[140:143], v6, s[20:23], s70 offen offset:1024 sc1\n"
      "buffer_load_dwordx4 v[144:147], v6, s[20:23], s70 offen offset:2048 sc1\n"
      "buffer_load_dwordx4 v[148:151], v6, s[20:23], s70 offen offset:3072 sc1\n"
      "buffer_load_dwordx4 v[152:155], v6, s[20:23], s71 offen offset:0 sc1\n"
      "buffer_load_dwordx4 v[156:159], v6, s[20:23], s71 offen offset:1024 sc1\n"
      "s_cmp_eq_u32 s48, 1\n"
      "s_cbranch_scc1 .Ll_ni\n"
      "s_barrier\n"
      ".Ll_ni:\n"
      "v_mov_b32 v0, v248\n"
      "v_xor_b32 v1, 128, v248\n"
      "s_waitcnt vmcnt(4)\n"
      "v_mfma_f32_16x16x32_bf16 v[192:195], v[8:11], v[136:139], 0\n"
      "v_mfma_f32_16x16x32_bf16 v[208:211], v[8:11], v[140:143], 0\n"
      "buffer_load_dwordx4 v[160:163], v6, s[20:23], s71 offen offset:2048 sc1\n"
      "buffer_load_dwordx4 v[164:167], v6, s[20:23], s71 offen offset:3072 sc1\n"
      "v_mfma_f32_16x16x32_bf16 v[196:199], v[12:15], v[136:139], 0\n"
      "v_mfma_f32_16x16x32_bf16 v[212:215], v[12:15], v[140:143], 0\n"
      "buffer_load_dwordx4 v[168:171], v6, s[20:23], s72 offen offset:0 sc1\n"
      "buffer_load_dwordx4 v[172:175], v6, s[20:23], s72 offen offset:1024 sc1\n"
      "v_mfma_f32_16x16x32_bf16 v[200:203], v[16:19], v[136:139], 0\n"
      "v_mfma_f32_16x16x32_bf16 v[216:219], v[16:19], v[140:143], 0\n"
      "v_mfma_f32_16x16x32_bf16 v[204:207], v[20:23], v[136:139], 0\n"
      "v_mfma_f32_16x16x32_bf16 v[220:223], v[20:23], v[140:143], 0\n"
      "buffer_load_dwordx4 v[136:139], v6, s[20:23], s72 offen offset:2048 sc1\n"
      "buffer_load_dwordx4 v[140:143], v6, s[20:23], s72 offen offset:3072 sc1\n"
      "s_waitcnt vmcnt(8)\n"
      "v_mfma_f32_16x16x32_bf16 v[192:195], v[24:27], v[144:147], v[192:195]\n"
      "v_mfma_f32_16x16x32_bf16 v[208:211], v[24:27], v[148:151], v[208:211]\n"
      "v_mfma_f32_16x16x32_bf16 v[196:199], v[28:31], v[144:147], v[196:199]\n"
      "v_mfma_f32_16x16x32_bf16 v[212:215], v[28:31], v[148:151], v[212:215]\n"
      "v_mfma_f32_16x16x32_bf16 v[200:203], v[32:35], v[144:147], v[200:203]\n"
      "v_mfma_f32_16x16x32_bf16 v[216:219], v[32:35], v[148:151], v[216:219]\n"
      "v_mfma_f32_16x16x32_bf16 v[204:207], v[36:39], v[144:147], v[204:207]\n"
      "v_mfma_f32_16x16x32_bf16 v[220:223], v[36:39], v[148:151], v[220:223]\n"
      "buffer_load_dwordx4 v[144:147], v6, s[20:23], s73 offen offset:0 sc1\n"
      "buffer_load_dwordx4 v[148:151], v6, s[20:23], s73 offen offset:1024 sc1\n"
      "s_waitcnt vmcnt(8)\n"
      "v_mfma_f32_16x16x32_bf16 v[192:195], v[40:43], v[152:155], v[192:195]\n"
      "v_mfma_f32_16x16x32_bf16 v[208:211], v[40:43], v[156:159], v[208:211]\n"
      "v_mfma_f32_16x16x32_bf16 v[196:199], v[44:47], v[152:155], v[196:199]\n"
      "v_mfma_f32_16x16x32_bf16 v[212:215], v[44:47], v[156:159], v[212:215]\n"
      "v_mfma_f32_16x16x32_bf16 v[200:203], v[48:51], v[152:155], v[200:203]\n"
      "v_mfma_f32_16x16x32_bf16 v[216:219], v[48:51], v[156:159], v[216:219]\n"
      "v_mfma_f32_16x16x32_bf16 v[204:207], v[52:55], v[152:155], v[204:207]\n"
      "v_mfma_f32_16x16x32_bf16 v[220:223], v[52:55], v[156:159], v[220:223]\n"
      "buffer_load_dwordx4 v[152:155], v6, s[20:23], s73 offen offset:2048 sc1\n"
      "buffer_load_dwordx4 v[156:159], v6, s[20:23], s73 offen offset:3072 sc1\n"
      "s_waitcnt vmcnt(8)\n"
      "v_mfma_f32_16x16x32_bf16 v[192:195], v[56:59], v[160:163], v[192:195]\n"
      "v_mfma_f32_16x16x32_bf16 v[208:211], v[56:59], v[164:167], v[208:211]\n"
      "v_mfma_f32_16x16x32_bf16 v[196:199], v[60:63], v[160:163], v[196:199]\n"
      "v_mfma_f32_16x16x32_bf16 v[212:215], v[60:63], v[164:167], v[212:215]\n"
      "v_mfma_f32_16x16x32_bf16 v[200:203], v[64:67], v[160:163], v[200:203]\n"
      "v_mfma_f32_16x16x32_bf16 v[216:219], v[64:67], v[164:167], v[216:219]\n"
      "v_mfma_f32_16x16x32_bf16 v[204:207], v[68:71], v[160:163], v[204:207]\n"
      "v_mfma_f32_16x16x32_bf16 v[220:223], v[68:71], v[164:167], v[220:223]\n"
      "buffer_load_dwordx4 v[160:163], v6, s[20:23], s74 offen offset:0 sc1\n"
      "buffer_load_dwordx4 v[164:167], v6, s[20:23], s74 offen offset:1024 sc1\n"
      "s_waitcnt vmcnt(8)\n"
      "v_mfma_f32_16x16x32_bf16 v[192:195], v[72:75], v[168:171], v[192:195]\n"
      "v_mfma_f32_16x16x32_bf16 v[208:211], v[72:75], v[172:175], v[208:211]\n"
      "v_mfma_f32_16x16x32_bf16 v[196:199], v[76:79], v[168:171], v[196:199]\n"
      "v_mfma_f32_16x16x32_bf16 v[212:215], v[76:79], v[172:175], v[212:215]\n"
      "v_mfma_f32_16x16x32_bf16 v[200:203], v[80:83], v[168:171], v[200:203]\n"
      "v_mfma_f32_16x16x32_bf16 v[216:219], v[80:83], v[172:175], v[216:219]\n"
      "v_mfma_f32_16x16x32_bf16 v[204:207], v[84:87], v[168:171], v[204:207]\n"
      "v_mfma_f32_16x16x32_bf16 v[220:223], v[84:87], v[172:175], v[220:223]\n"
      "buffer_load_dwordx4 v[168:171], v6, s[20:23], s74 offen offset:2048 sc1\n"
      "buffer_load_dwordx4 v[172:175], v6, s[20:23], s74 offen offset:3072 sc1\n"
      "s_waitcnt vmcnt(8)\n"
      "v_mfma_f32_16x16x32_bf16 v[192:195], v[88:91], v[136:139], v[192:195]\n"
      "v_mfma_f32_16x16x32_bf16 v[208:211], v[88:91], v[140:143], v[208:211]\n"
      "v_mfma_f32_16x16x32_bf16 v[196:199], v[92:95], v[136:139], v[196:199]\n"
      "v_mfma_f32_16x16x32_bf16 v[212:215], v[92:95], v[140:143], v[212:215]\n"
      "v_mfma_f32_16x16x32_bf16 v[200:203], v[96:99], v[136:139], v[200:203]\n"
      "v_mfma_f32_16x16x32_bf16 v[216:219], v[96:99], v[140:143], v[216:219]\n"
      "v_mfma_f32_16x16x32_bf16 v[204:207], v[100:103], v[136:139], v[204:207]\n"
      "v_mfma_f32_16x16x32_bf16 v[220:223], v[100:103], v[140:143], v[220:223]\n"
      "buffer_load_dwordx4 v[136:139], v6, s[20:23], s75 offen offset:0 sc1\n"
      "buffer_load_dwordx4 v[140:143], v6, s[20:23], s75 offen offset:1024 sc1\n"
      "s_waitcnt vmcnt(8)\n"
      "v_mfma_f32_16x16x32_bf16 v[192:195], v[104:107], v[144:147], v[192:195]\n"
      "v_add_u32 v2, 32768, v2\n"
      "ds_read_b64_tr_b16 v[176:177], v2 offset:32768\n"
      "ds_read_b64_tr_b16 v[178:179], v2 offset:34816\n"
      "v_mfma_f32_16x16x32_bf16 v[208:211], v[104:107], v[148:151], v[208:211]\n"
      "v_mfma_f32_16x16x32_bf16 v[196:199], v[108:111], v[144:147], v[196:199]\n"
      "v_add_u32 v3, 32768, v3\n"
      "ds_read_b64_tr_b16 v[180:181], v3 offset:32768\n"
      "ds_read_b64_tr_b16 v[182:183], v3 offset:34816\n"
      "v_mfma_f32_16x16x32_bf16 v[212:215], v[108:111], v[148:151], v[212:215]\n"
      "v_mfma_f32_16x16x32_bf16 v[200:203], v[112:115], v[144:147], v[200:203]\n"
      "v_add_u32 v4, 32768, v4\n"
      "ds_read_b64_tr_b16 v[184:185], v4 offset:32768\n"
      "ds_read_b64_tr_b16 v[186:187], v4 offset:34816\n"
      "v_mfma_f32_16x16x32_bf16 v[216:219], v[112:115], v[148:151], v[216:219]\n"
      "v_mfma_f32_16x16x32_bf16 v[204:207], v[116:119], v[144:147], v[204:207]\n"
      "v_add_u32 v5, 32768, v5\n"
      "ds_read_b64_tr_b16 v[188:189], v5 offset:32768\n"
      "ds_read_b64_tr_b16 v[190:191], v5 offset:34816\n"
      "v_mfma_f32_16x16x32_bf16 v[220:223], v[116:119], v[148:151], v[220:223]\n"
      "buffer_load_dwordx4 v[144:147], v6, s[20:23], s75 offen offset:2048 sc1\n"
      "buffer_load_dwordx4 v[148:151], v6, s[20:23], s75 offen offset:3072 sc1\n"
      "s_waitcnt vmcnt(8)\n"
      "v_mfma_f32_16x16x32_bf16 v[192:195], v[120:123], v[152:155], v[192:195]\n"
      "v_mfma_f32_16x16x32_bf16 v[208:211], v[120:123], v[156:159], v[208:211]\n"
      "v_mfma_f32_16x16x32_bf16 v[196:199], v[124:127], v[152:155], v[196:199]\n"
      "v_mfma_f32_16x16x32_bf16 v[212:215], v[124:127], v[156:159], v[212:215]\n"
      "v_mfma_f32_16x16x32_bf16 v[200:203], v[128:131], v[152:155], v[200:203]\n"
      "v_mfma_f32_16x16x32_bf16 v[216:219], v[128:131], v[156:159], v[216:219]\n"
      "v_mfma_f32_16x16x32_bf16 v[204:207], v[132:135], v[152:155], v[204:207]\n"
      "v_mfma_f32_16x16x32_bf16 v[220:223], v[132:135], v[156:159], v[220:223]\n"
      "buffer_load_dwordx4 v[152:155], v6, s[20:23], s76 offen offset:0 sc1\n"
      "buffer_load_dwordx4 v[156:159], v6, s[20:23], s76 offen offset:1024 sc1\n"
      "s_waitcnt vmcnt(8)\n"
      "s_waitcnt lgkmcnt(6)\n"
      "v_mfma_f32_16x16x32_bf16 v[192:195], v[176:179], v[160:163], v[192:195]\n"
      "v_mfma_f32_16x16x32_bf16 v[208:211], v[176:179], v[164:167], v[208:211]\n"
      "s_waitcnt lgkmcnt(4)\n"
      "v_mfma_f32_16x16x32_bf16 v[196:199], v[180:183], v[160:163], v[196:199]\n"
      "ds_read_b64_tr_b16 v[176:177], v2 offset:36864\n"
      "ds_read_b64_tr_b16 v[178:179], v2 offset:38912\n"
      "v_mfma_f32_16x16x32_bf16 v[212:215], v[180:183], v[164:167], v[212:215]\n"
      "s_waitcnt lgkmcnt(4)\n"
      "v_mfma_f32_16x16x32_bf16 v[200:203], v[184:187], v[160:163], v[200:203]\n"
      "ds_read_b64_tr_b16 v[180:181], v3 offset:36864\n"
      "ds_read_b64_tr_b16 v[182:183], v3 offset:38912\n"
      "v_mfma_f32_16x16x32_bf16 v[216:219], v[184:187], v[164:167], v[216:219]\n"
      "s_waitcnt lgkmcnt(4)\n"
      "v_mfma_f32_16x16x32_bf16 v[204:207], v[188:191], v[160:163], v[204:207]\n"
      "ds_read_b64_tr_b16 v[184:185], v4 offset:36864\n"
      "ds_read_b64_tr_b16 v[186:187], v4 offset:38912\n"
      "v_mfma_f32_16x16x32_bf16 v[220:223], v[188:191], v[164:167], v[220:223]\n"
      "buffer_load_dwordx4 v[160:163], v6, s[20:23], s76 offen offset:2048 sc1\n"
      "buffer_load_dwordx4 v[164:167], v6, s[20:23], s76 offen offset:3072 sc1\n"
      "s_waitcnt vmcnt(8)\n"
      "s_waitcnt lgkmcnt(4)\n"
      "v_mfma_f32_16x16x32_bf16 v[192:195], v[176:179], v[168:171], v[192:195]\n"
      "ds_read_b64_tr_b16 v[188:189], v5 offset:36864\n"
      "ds_read_b64_tr_b16 v[190:191], v5 offset:38912\n"
      "v_mfma_f32_16x16x32_bf16 v[208:211], v[176:179], v[172:175], v[208:211]\n"
      "s_waitcnt lgkmcnt(4)\n"
      "v_mfma_f32_16x16x32_bf16 v[196:199], v[180:183], v[168:171], v[196:199]\n"
      "v_add_u32 v2, 4294934528, v2\n"
      "ds_read_b64_tr_b16 v[176:177], v2 offset:0\n"
      "ds_read_b64_tr_b16 v[178:179], v2 offset:2048\n"
      "v_mfma_f32_16x16x32_bf16 v[212:215], v[180:183], v[172:175], v[212:215]\n"
      "s_waitcnt lgkmcnt(4)\n"
      "v_mfma_f32_16x16x32_bf16 v[200:203], v[184:187], v[168:171], v[200:203]\n"
      "v_add_u32 v3, 4294934528, v3\n"
      "ds_read_b64_tr_b16 v[180:181], v3 offset:0\n"
      "ds_read_b64_tr_b16 v[182:183], v3 offset:2048\n"
      "v_mfma_f32_16x16x32_bf16 v[216:219], v[184:187], v[172:175], v[216:219]\n"
      "s_waitcnt lgkmcnt(4)\n"
      "v_mfma_f32_16x16x32_bf16 v[204:207], v[188:191], v[168:171], v[204:207]\n"
      "v_add_u32 v4, 4294934528, v4\n"
      "ds_read_b64_tr_b16 v[184:185], v4 offset:0\n"
      "ds_read_b64_tr_b16 v[186:187], v4 offset:2048\n"
      "v_mfma_f32_16x16x32_bf16 v[220:223], v[188:191], v[172:175], v[220:223]\n"
      "s_waitcnt vmcnt(6)\n"
      "s_waitcnt lgkmcnt(4)\n"
      "v_mfma_f32_16x16x32_bf16 v[192:195], v[176:179], v[136:139], v[192:195]\n"
      "v_add_u32 v5, 4294934528, v5\n"
      "ds_read_b64_tr_b16 v[188:189], v5 offset:0\n"
      "ds_read_b64_tr_b16 v[190:191], v5 offset:2048\n"
      "v_mfma_f32_16x16x32_bf16 v[208:211], v[176:179], v[140:143], v[208:211]\n"
      "s_barrier\n"
      "v_add_u32 v6, 231424, v6\n"
      "buffer_load_dwordx4 v[168:171], v6, s[20:23], s70 offen offset:0 sc1\n"
      "buffer_load_dwordx4 v[172:175], v6, s[20:23], s70 offen offset:1024 sc1\n"
      "s_waitcnt lgkmcnt(4)\n"
      "v_mfma_f32_16x16x32_bf16 v[196:199], v[180:183], v[136:139], v[196:199]\n"
      "ds_read_b64_tr_b16 v[176:177], v2 offset:4096\n"
      "ds_read_b64_tr_b16 v[178:179], v2 offset:6144\n"
      "v_mfma_f32_16x16x32_bf16 v[212:215], v[180:183], v[140:143], v[212:215]\n"
      "s_waitcnt lgkmcnt(4)\n"
      "v_mfma_f32_16x16x32_bf16 v[200:203], v[184:187], v[136:139], v[200:203]\n"
      "ds_read_b64_tr_b16 v[180:181], v3 offset:4096\n"
      "ds_read_b64_tr_b16 v[182:183], v3 offset:6144\n"
      "v_mfma_f32_16x16x32_bf16 v[216:219], v[184:187], v[140:143], v[216:219]\n"
      "s_waitcnt lgkmcnt(4)\n"
      "v_mfma_f32_16x16x32_bf16 v[204:207], v[188:191], v[136:139], v[204:207]\n"
      "ds_read_b64_tr_b16 v[184:185], v4 offset:4096\n"
      "ds_read_b64_tr_b16 v[186:187], v4 offset:6144\n"
      "v_mfma_f32_16x16x32_bf16 v[220:223], v[188:191], v[140:143], v[220:223]\n"
      "buffer_load_dwordx4 v[136:139], v6, s[20:23], s70 offen offset:2048 sc1\n"
      "buffer_load_dwordx4 v[140:143], v6, s[20:23], s70 offen offset:3072 sc1\n"
      "s_waitcnt vmcnt(8)\n"
      "s_waitcnt lgkmcnt(4)\n"
      "v_mfma_f32_16x16x32_bf16 v[192:195], v[176:179], v[144:147], v[192:195]\n"
      "ds_read_b64_tr_b16 v[188:189], v5 offset:4096\n"
      "ds_read_b64_tr_b16 v[190:191], v5 offset:6144\n"
      "v_mfma_f32_16x16x32_bf16 v[208:211], v[176:179], v[148:151], v[208:211]\n"
      "s_waitcnt lgkmcnt(4)\n"
      "v_mfma_f32_16x16x32_bf16 v[196:199], v[180:183], v[144:147], v[196:199]\n"
      "ds_read_b64_tr_b16 v[176:177], v2 offset:32768\n"
      "ds_read_b64_tr_b16 v[178:179], v2 offset:34816\n"
      "v_mfma_f32_16x16x32_bf16 v[212:215], v[180:183], v[148:151], v[212:215]\n"
      "s_waitcnt lgkmcnt(4)\n"
      "v_mfma_f32_16x16x32_bf16 v[200:203], v[184:187], v[144:147], v[200:203]\n"
      "ds_read_b64_tr_b16 v[180:181], v3 offset:32768\n"
      "ds_read_b64_tr_b16 v[182:183], v3 offset:34816\n"
      "v_mfma_f32_16x16x32_bf16 v[216:219], v[184:187], v[148:151], v[216:219]\n"
      "s_waitcnt lgkmcnt(4)\n"
      "v_mfma_f32_16x16x32_bf16 v[204:207], v[188:191], v[144:147], v[204:207]\n"
      "ds_read_b64_tr_b16 v[184:185], v4 offset:32768\n"
      "ds_read_b64_tr_b16 v[186:187], v4 offset:34816\n"
      "v_mfma_f32_16x16x32_bf16 v[220:223], v[188:191], v[148:151], v[220:223]\n"
      "buffer_load_dwordx4 v[144:147], v6, s[20:23], s71 offen offset:0 sc1\n"
      "buffer_load_dwordx4 v[148:151], v6, s[20:23], s71 offen offset:1024 sc1\n"
      "s_waitcnt vmcnt(8)\n"
      "s_waitcnt lgkmcnt(4)\n"
      "v_mfma_f32_16x16x32_bf16 v[192:195], v[176:179], v[152:155], v[192:195]\n"
      "ds_read_b64_tr_b16 v[188:189], v5 offset:32768\n"
      "ds_read_b64_tr_b16 v[190:191], v5 offset:34816\n"
      "v_mfma_f32_16x16x32_bf16 v[208:211], v[176:179], v[156:159], v[208:211]\n"
      "s_waitcnt lgkmcnt(4)\n"
      "v_mfma_f32_16x16x32_bf16 v[196:199], v[180:183], v[152:155], v[196:199]\n"
      "ds_read_b64_tr_b16 v[176:177], v2 offset:36864\n"
      "ds_read_b64_tr_b16 v[178:179], v2 offset:38912\n"
      "v_mfma_f32_16x16x32_bf16 v[212:215], v[180:183], v[156:159], v[212:215]\n"
      "s_waitcnt lgkmcnt(4)\n"
      "v_mfma_f32_16x16x32_bf16 v[200:203], v[184:187], v[152:155], v[200:203]\n"
      "ds_read_b64_tr_b16 v[180:181], v3 offset:36864\n"
      "ds_read_b64_tr_b16 v[182:183], v3 offset:38912\n"
      "v_mfma_f32_16x16x32_bf16 v[216:219], v[184:187], v[156:159], v[216:219]\n"
      "s_waitcnt lgkmcnt(4)\n"
      "v_mfma_f32_16x16x32_bf16 v[204:207], v[188:191], v[152:155], v[204:207]\n"
      "ds_read_b64_tr_b16 v[184:185], v4 offset:36864\n"
      "ds_read_b64_tr_b16 v[186:187], v4 offset:38912\n"
      "v_mfma_f32_16x16x32_bf16 v[220:223], v[188:191], v[156:159], v[220:223]\n"
      "buffer_load_dwordx4 v[152:155], v6, s[20:23], s71 offen offset:2048 sc1\n"
      "buffer_load_dwordx4 v[156:159], v6, s[20:23], s71 offen offset:3072 sc1\n"
      "s_waitcnt vmcnt(8)\n"
      "s_waitcnt lgkmcnt(4)\n"
      "v_mfma_f32_16x16x32_bf16 v[192:195], v[176:179], v[160:163], v[192:195]\n"
      "ds_read_b64_tr_b16 v[188:189], v5 offset:36864\n"
      "ds_read_b64_tr_b16 v[190:191], v5 offset:38912\n"
      "v_mfma_f32_16x16x32_bf16 v[208:211], v[176:179], v[164:167], v[208:211]\n"
      "s_waitcnt lgkmcnt(4)\n"
      "v_mfma_f32_16x16x32_bf16 v[196:199], v[180:183], v[160:163], v[196:199]\n"
      "v_mfma_f32_16x16x32_bf16 v[212:215], v[180:183], v[164:167], v[212:215]\n"
      "s_waitcnt lgkmcnt(2)\n"
      "v_mfma_f32_16x16x32_bf16 v[200:203], v[184:187], v[160:163], v[200:203]\n"
      "v_mfma_f32_16x16x32_bf16 v[216:219], v[184:187], v[164:167], v[216:219]\n"
      "s_waitcnt lgkmcnt(0)\n"
      "v_mfma_f32_16x16x32_bf16 v[204:207], v[188:191], v[160:163], v[204:207]\n"
      "v_mfma_f32_16x16x32_bf16 v[220:223], v[188:191], v[164:167], v[220:223]\n"
      "buffer_load_dwordx4 v[160:163], v6, s[20:23], s74 offen offset:0 sc1\n"
      "buffer_load_dwordx4 v[164:167], v6, s[20:23], s74 offen offset:1024 sc1\n"
      "s_waitcnt vmcnt(8)\n"
      "v_mfma_f32_16x16x32_bf16 v[224:227], v[8:11], v[168:171], 0\n"
      "v_mfma_f32_16x16x32_bf16 v[240:243], v[8:11], v[172:175], 0\n"
      "v_mfma_f32_16x16x32_bf16 v[228:231], v[12:15], v[168:171], 0\n"
      "v_mfma_f32_16x16x32_bf16 v[244:247], v[12:15], v[172:175], 0\n"
      "v_mfma_f32_16x16x32_bf16 v[232:235], v[16:19], v[168:171], 0\n"
      "v_mfma_f32_16x16x32_bf16 v[248:251], v[16:19], v[172:175], 0\n"
      "v_mfma_f32_16x16x32_bf16 v[236:239], v[20:23], v[168:171], 0\n"
      "v_mfma_f32_16x16x32_bf16 v[252:255], v[20:23], v[172:175], 0\n"
      "buffer_load_dwordx4 v[168:171], v6, s[20:23], s74 offen offset:2048 sc1\n"
      "buffer_load_dwordx4 v[172:175], v6, s[20:23], s74 offen offset:3072 sc1\n"
      "s_waitcnt vmcnt(8)\n"
      "v_mfma_f32_16x16x32_bf16 v[224:227], v[24:27], v[136:139], v[224:227]\n"
      "v_mfma_f32_16x16x32_bf16 v[240:243], v[24:27], v[140:143], v[240:243]\n"
      "ds_write_b128 v0, v[192:195] offset:0\n"
      "v_mfma_f32_16x16x32_bf16 v[228:231], v[28:31], v[136:139], v[228:231]\n"
      "v_mfma_f32_16x16x32_bf16 v[244:247], v[28:31], v[140:143], v[244:247]\n"
      "ds_write_b128 v1, v[196:199] offset:1024\n"
      "v_mfma_f32_16x16x32_bf16 v[232:235], v[32:35], v[136:139], v[232:235]\n"
      "v_mfma_f32_16x16x32_bf16 v[248:251], v[32:35], v[140:143], v[248:251]\n"
      "v_mfma_f32_16x16x32_bf16 v[236:239], v[36:39], v[136:139], v[236:239]\n"
      "v_mfma_f32_16x16x32_bf16 v[252:255], v[36:39], v[140:143], v[252:255]\n"
      "buffer_load_dwordx4 v[136:139], v6, s[20:23], s75 offen offset:0 sc1\n"
      "buffer_load_dwordx4 v[140:143], v6, s[20:23], s75 offen offset:1024 sc1\n"
      "s_waitcnt vmcnt(8)\n"
      "v_mfma_f32_16x16x32_bf16 v[224:227], v[40:43], v[144:147], v[224:227]\n"
      "v_add_u32 v2, 32768, v2\n"
      "ds_read_b64_tr_b16 v[176:177], v2 offset:32768\n"
      "ds_read_b64_tr_b16 v[178:179], v2 offset:34816\n"
      "v_mfma_f32_16x16x32_bf16 v[240:243], v[40:43], v[148:151], v[240:243]\n"
      "ds_write_b128 v0, v[200:203] offset:2048\n"
      "v_mfma_f32_16x16x32_bf16 v[228:231], v[44:47], v[144:147], v[228:231]\n"
      "v_add_u32 v3, 32768, v3\n"
      "ds_read_b64_tr_b16 v[180:181], v3 offset:32768\n"
      "ds_read_b64_tr_b16 v[182:183], v3 offset:34816\n"
      "v_mfma_f32_16x16x32_bf16 v[244:247], v[44:47], v[148:151], v[244:247]\n"
      "ds_write_b128 v1, v[204:207] offset:3072\n"
      "v_mfma_f32_16x16x32_bf16 v[232:235], v[48:51], v[144:147], v[232:235]\n"
      "v_add_u32 v4, 32768, v4\n"
      "ds_read_b64_tr_b16 v[184:185], v4 offset:32768\n"
      "ds_read_b64_tr_b16 v[186:187], v4 offset:34816\n"
      "v_mfma_f32_16x16x32_bf16 v[248:251], v[48:51], v[148:151], v[248:251]\n"
      "v_mfma_f32_16x16x32_bf16 v[236:239], v[52:55], v[144:147], v[236:239]\n"
      "v_add_u32 v5, 32768, v5\n"
      "ds_read_b64_tr_b16 v[188:189], v5 offset:32768\n"
      "ds_read_b64_tr_b16 v[190:191], v5 offset:34816\n"
      "v_mfma_f32_16x16x32_bf16 v[252:255], v[52:55], v[148:151], v[252:255]\n"
      "buffer_load_dwordx4 v[144:147], v6, s[20:23], s75 offen offset:2048 sc1\n"
      "buffer_load_dwordx4 v[148:151], v6, s[20:23], s75 offen offset:3072 sc1\n"
      "s_waitcnt vmcnt(8)\n"
      "v_mfma_f32_16x16x32_bf16 v[224:227], v[56:59], v[152:155], v[224:227]\n"
      "v_mfma_f32_16x16x32_bf16 v[240:243], v[56:59], v[156:159], v[240:243]\n"
      "ds_write_b128 v0, v[208:211] offset:4096\n"
      "v_mfma_f32_16x16x32_bf16 v[228:231], v[60:63], v[152:155], v[228:231]\n"
      "v_mfma_f32_16x16x32_bf16 v[244:247], v[60:63], v[156:159], v[244:247]\n"
      "ds_write_b128 v1, v[212:215] offset:5120\n"
      "v_mfma_f32_16x16x32_bf16 v[232:235], v[64:67], v[152:155], v[232:235]\n"
      "v_mfma_f32_16x16x32_bf16 v[248:251], v[64:67], v[156:159], v[248:251]\n"
      "v_mfma_f32_16x16x32_bf16 v[236:239], v[68:71], v[152:155], v[236:239]\n"
      "v_mfma_f32_16x16x32_bf16 v[252:255], v[68:71], v[156:159], v[252:255]\n"
      "buffer_load_dwordx4 v[152:155], v6, s[20:23], s76 offen offset:0 sc1\n"
      "buffer_load_dwordx4 v[156:159], v6, s[20:23], s76 offen offset:1024 sc1\n"
      "s_waitcnt vmcnt(8)\n"
      "s_waitcnt lgkmcnt(10)\n"
      "v_mfma_f32_16x16x32_bf16 v[224:227], v[176:179], v[160:163], v[224:227]\n"
      "v_mfma_f32_16x16x32_bf16 v[240:243], v[176:179], v[164:167], v[240:243]\n"
      "ds_write_b128 v0, v[216:219] offset:6144\n"
      "s_waitcnt lgkmcnt(8)\n"
      "v_mfma_f32_16x16x32_bf16 v[228:231], v[180:183], v[160:163], v[228:231]\n"
      "ds_read_b64_tr_b16 v[176:177], v2 offset:36864\n"
      "ds_read_b64_tr_b16 v[178:179], v2 offset:38912\n"
      "v_mfma_f32_16x16x32_bf16 v[244:247], v[180:183], v[164:167], v[244:247]\n"
      "ds_write_b128 v1, v[220:223] offset:7168\n"
      "s_waitcnt lgkmcnt(8)\n"
      "v_mfma_f32_16x16x32_bf16 v[232:235], v[184:187], v[160:163], v[232:235]\n"
      "ds_read_b64_tr_b16 v[180:181], v3 offset:36864\n"
      "ds_read_b64_tr_b16 v[182:183], v3 offset:38912\n"
      "v_mfma_f32_16x16x32_bf16 v[248:251], v[184:187], v[164:167], v[248:251]\n"
      "s_waitcnt lgkmcnt(8)\n"
      "v_mfma_f32_16x16x32_bf16 v[236:239], v[188:191], v[160:163], v[236:239]\n"
      "ds_read_b64_tr_b16 v[184:185], v4 offset:36864\n"
      "ds_read_b64_tr_b16 v[186:187], v4 offset:38912\n"
      "v_mfma_f32_16x16x32_bf16 v[252:255], v[188:191], v[164:167], v[252:255]\n"
      "buffer_load_dwordx4 v[160:163], v6, s[20:23], s76 offen offset:2048 sc1\n"
      "buffer_load_dwordx4 v[164:167], v6, s[20:23], s76 offen offset:3072 sc1\n"
      "s_waitcnt vmcnt(8)\n"
      "s_waitcnt lgkmcnt(5)\n"
      "v_mfma_f32_16x16x32_bf16 v[224:227], v[176:179], v[168:171], v[224:227]\n"
      "ds_read_b64_tr_b16 v[188:189], v5 offset:36864\n"
      "ds_read_b64_tr_b16 v[190:191], v5 offset:38912\n"
      "v_mfma_f32_16x16x32_bf16 v[240:243], v[176:179], v[172:175], v[240:243]\n"
      "s_waitcnt lgkmcnt(4)\n"
      "v_mfma_f32_16x16x32_bf16 v[228:231], v[180:183], v[168:171], v[228:231]\n"
      "v_add_u32 v2, 4294934528, v2\n"
      "ds_read_b64_tr_b16 v[176:177], v2 offset:0\n"
      "ds_read_b64_tr_b16 v[178:179], v2 offset:2048\n"
      "v_mfma_f32_16x16x32_bf16 v[244:247], v[180:183], v[172:175], v[244:247]\n"
      "s_waitcnt lgkmcnt(4)\n"
      "v_mfma_f32_16x16x32_bf16 v[232:235], v[184:187], v[168:171], v[232:235]\n"
      "v_add_u32 v3, 4294934528, v3\n"
      "ds_read_b64_tr_b16 v[180:181], v3 offset:0\n"
      "ds_read_b64_tr_b16 v[182:183], v3 offset:2048\n"
      "v_mfma_f32_16x16x32_bf16 v[248:251], v[184:187], v[172:175], v[248:251]\n"
      "s_waitcnt lgkmcnt(4)\n"
      "v_mfma_f32_16x16x32_bf16 v[236:239], v[188:191], v[168:171], v[236:239]\n"
      "v_add_u32 v4, 4294934528, v4\n"
      "ds_read_b64_tr_b16 v[184:185], v4 offset:0\n"
      "ds_read_b64_tr_b16 v[186:187], v4 offset:2048\n"
      "v_mfma_f32_16x16x32_bf16 v[252:255], v[188:191], v[172:175], v[252:255]\n"
      "buffer_load_dwordx4 v[168:171], v6, s[20:23], s72 offen offset:0 sc1\n"
      "buffer_load_dwordx4 v[172:175], v6, s[20:23], s72 offen offset:2048 sc1\n"
      "s_waitcnt lgkmcnt(0)\n"
      "s_barrier\n"
      "v_subrev_u32 v0, 131072, v0\n"
      "v_subrev_u32 v1, 131072, v1\n"
      "s_waitcnt vmcnt(8)\n"
      "s_waitcnt lgkmcnt(4)\n"
      "v_mfma_f32_16x16x32_bf16 v[224:227], v[176:179], v[136:139], v[224:227]\n"
      "v_add_u32 v5, 4294934528, v5\n"
      "ds_read_b64_tr_b16 v[188:189], v5 offset:0\n"
      "ds_read_b64_tr_b16 v[190:191], v5 offset:2048\n"
      "v_mfma_f32_16x16x32_bf16 v[240:243], v[176:179], v[140:143], v[240:243]\n"
      "s_waitcnt lgkmcnt(4)\n"
      "v_mfma_f32_16x16x32_bf16 v[228:231], v[180:183], v[136:139], v[228:231]\n"
      "ds_read_b64_tr_b16 v[176:177], v2 offset:4096\n"
      "ds_read_b64_tr_b16 v[178:179], v2 offset:6144\n"
      "v_mfma_f32_16x16x32_bf16 v[244:247], v[180:183], v[140:143], v[244:247]\n"
      "s_waitcnt lgkmcnt(4)\n"
      "v_mfma_f32_16x16x32_bf16 v[232:235], v[184:187], v[136:139], v[232:235]\n"
      "ds_read_b64_tr_b16 v[180:181], v3 offset:4096\n"
      "ds_read_b64_tr_b16 v[182:183], v3 offset:6144\n"
      "v_mfma_f32_16x16x32_bf16 v[248:251], v[184:187], v[140:143], v[248:251]\n"
      "s_waitcnt lgkmcnt(4)\n"
      "v_mfma_f32_16x16x32_bf16 v[236:239], v[188:191], v[136:139], v[236:239]\n"
      "ds_read_b64_tr_b16 v[184:185], v4 offset:4096\n"
      "ds_read_b64_tr_b16 v[186:187], v4 offset:6144\n"
      "v_mfma_f32_16x16x32_bf16 v[252:255], v[188:191], v[140:143], v[252:255]\n"
      "buffer_load_dwordx4 v[136:139], v6, s[20:23], s73 offen offset:0 sc1\n"
      "buffer_load_dwordx4 v[140:143], v6, s[20:23], s73 offen offset:2048 sc1\n"
      "s_waitcnt vmcnt(8)\n"
      "s_waitcnt lgkmcnt(4)\n"
      "v_mfma_f32_16x16x32_bf16 v[224:227], v[176:179], v[144:147], v[224:227]\n"
      "ds_read_b64_tr_b16 v[188:189], v5 offset:4096\n"
      "ds_read_b64_tr_b16 v[190:191], v5 offset:6144\n"
      "v_mfma_f32_16x16x32_bf16 v[240:243], v[176:179], v[148:151], v[240:243]\n"
      "s_waitcnt lgkmcnt(4)\n"
      "v_mfma_f32_16x16x32_bf16 v[228:231], v[180:183], v[144:147], v[228:231]\n"
      "ds_read_b64_tr_b16 v[176:177], v2 offset:32768\n"
      "ds_read_b64_tr_b16 v[178:179], v2 offset:34816\n"
      "v_mfma_f32_16x16x32_bf16 v[244:247], v[180:183], v[148:151], v[244:247]\n"
      "s_waitcnt lgkmcnt(4)\n"
      "v_mfma_f32_16x16x32_bf16 v[232:235], v[184:187], v[144:147], v[232:235]\n"
      "ds_read_b64_tr_b16 v[180:181], v3 offset:32768\n"
      "ds_read_b64_tr_b16 v[182:183], v3 offset:34816\n"
      "v_mfma_f32_16x16x32_bf16 v[248:251], v[184:187], v[148:151], v[248:251]\n"
      "s_waitcnt lgkmcnt(4)\n"
      "v_mfma_f32_16x16x32_bf16 v[236:239], v[188:191], v[144:147], v[236:239]\n"
      "ds_read_b64_tr_b16 v[184:185], v4 offset:32768\n"
      "ds_read_b64_tr_b16 v[186:187], v4 offset:34816\n"
      "v_mfma_f32_16x16x32_bf16 v[252:255], v[188:191], v[148:151], v[252:255]\n"
      "buffer_load_dwordx4 v[144:147], v6, s[20:23], s72 offen offset:1024 sc1\n"
      "buffer_load_dwordx4 v[148:151], v6, s[20:23], s72 offen offset:3072 sc1\n"
      "s_waitcnt vmcnt(8)\n"
      "s_waitcnt lgkmcnt(4)\n"
      "v_mfma_f32_16x16x32_bf16 v[224:227], v[176:179], v[152:155], v[224:227]\n"
      "ds_read_b64_tr_b16 v[188:189], v5 offset:32768\n"
      "ds_read_b64_tr_b16 v[190:191], v5 offset:34816\n"
      "v_mfma_f32_16x16x32_bf16 v[240:243], v[176:179], v[156:159], v[240:243]\n"
      "s_waitcnt lgkmcnt(4)\n"
      "v_mfma_f32_16x16x32_bf16 v[228:231], v[180:183], v[152:155], v[228:231]\n"
      "ds_read_b64_tr_b16 v[176:177], v2 offset:36864\n"
      "ds_read_b64_tr_b16 v[178:179], v2 offset:38912\n"
      "v_mfma_f32_16x16x32_bf16 v[244:247], v[180:183], v[156:159], v[244:247]\n"
      "s_waitcnt lgkmcnt(4)\n"
      "v_mfma_f32_16x16x32_bf16 v[232:235], v[184:187], v[152:155], v[232:235]\n"
      "ds_read_b64_tr_b16 v[180:181], v3 offset:36864\n"
      "ds_read_b64_tr_b16 v[182:183], v3 offset:38912\n"
      "v_mfma_f32_16x16x32_bf16 v[248:251], v[184:187], v[156:159], v[248:251]\n"
      "s_waitcnt lgkmcnt(4)\n"
      "v_mfma_f32_16x16x32_bf16 v[236:239], v[188:191], v[152:155], v[236:239]\n"
      "ds_read_b64_tr_b16 v[184:185], v4 offset:36864\n"
      "ds_read_b64_tr_b16 v[186:187], v4 offset:38912\n"
      "v_mfma_f32_16x16x32_bf16 v[252:255], v[188:191], v[156:159], v[252:255]\n"
      "buffer_load_dwordx4 v[152:155], v6, s[20:23], s73 offen offset:1024 sc1\n"
      "buffer_load_dwordx4 v[156:159], v6, s[20:23], s73 offen offset:3072 sc1\n"
      "s_waitcnt vmcnt(8)\n"
      "s_waitcnt lgkmcnt(4)\n"
      "v_mfma_f32_16x16x32_bf16 v[224:227], v[176:179], v[160:163], v[224:227]\n"
      "ds_read_b64_tr_b16 v[188:189], v5 offset:36864\n"
      "ds_read_b64_tr_b16 v[190:191], v5 offset:38912\n"
      "v_mfma_f32_16x16x32_bf16 v[240:243], v[176:179], v[164:167], v[240:243]\n"
      "s_waitcnt lgkmcnt(4)\n"
      "v_mfma_f32_16x16x32_bf16 v[228:231], v[180:183], v[160:163], v[228:231]\n"
      "v_mfma_f32_16x16x32_bf16 v[244:247], v[180:183], v[164:167], v[244:247]\n"
      "s_waitcnt lgkmcnt(2)\n"
      "v_mfma_f32_16x16x32_bf16 v[232:235], v[184:187], v[160:163], v[232:235]\n"
      "v_mfma_f32_16x16x32_bf16 v[248:251], v[184:187], v[164:167], v[248:251]\n"
      "s_waitcnt lgkmcnt(0)\n"
      "v_mfma_f32_16x16x32_bf16 v[236:239], v[188:191], v[160:163], v[236:239]\n"
      "v_mfma_f32_16x16x32_bf16 v[252:255], v[188:191], v[164:167], v[252:255]\n"
      "s_waitcnt vmcnt(6)\n"
      "v_mfma_f32_16x16x32_bf16 v[224:227], v[72:75], v[168:171], v[224:227]\n"
      "v_mfma_f32_16x16x32_bf16 v[228:231], v[76:79], v[168:171], v[228:231]\n"
      "v_mfma_f32_16x16x32_bf16 v[232:235], v[80:83], v[168:171], v[232:235]\n"
      "v_mfma_f32_16x16x32_bf16 v[236:239], v[84:87], v[168:171], v[236:239]\n"
      "v_mfma_f32_16x16x32_bf16 v[224:227], v[88:91], v[172:175], v[224:227]\n"
      "v_mfma_f32_16x16x32_bf16 v[228:231], v[92:95], v[172:175], v[228:231]\n"
      "v_mfma_f32_16x16x32_bf16 v[232:235], v[96:99], v[172:175], v[232:235]\n"
      "v_mfma_f32_16x16x32_bf16 v[236:239], v[100:103], v[172:175], v[236:239]\n"
      "s_waitcnt vmcnt(4)\n"
      "v_mfma_f32_16x16x32_bf16 v[224:227], v[104:107], v[136:139], v[224:227]\n"
      "v_mfma_f32_16x16x32_bf16 v[228:231], v[108:111], v[136:139], v[228:231]\n"
      "v_mfma_f32_16x16x32_bf16 v[232:235], v[112:115], v[136:139], v[232:235]\n"
      "v_mfma_f32_16x16x32_bf16 v[236:239], v[116:119], v[136:139], v[236:239]\n"
      "v_mfma_f32_16x16x32_bf16 v[224:227], v[120:123], v[140:143], v[224:227]\n"
      "v_mfma_f32_16x16x32_bf16 v[228:231], v[124:127], v[140:143], v[228:231]\n"
      "v_mfma_f32_16x16x32_bf16 v[232:235], v[128:131], v[140:143], v[232:235]\n"
      "v_mfma_f32_16x16x32_bf16 v[236:239], v[132:135], v[140:143], v[236:239]\n"
      "s_waitcnt vmcnt(2)\n"
      "v_mfma_f32_16x16x32_bf16 v[240:243], v[72:75], v[144:147], v[240:243]\n"
      "v_mfma_f32_16x16x32_bf16 v[244:247], v[76:79], v[144:147], v[244:247]\n"
      "v_mfma_f32_16x16x32_bf16 v[248:251], v[80:83], v[144:147], v[248:251]\n"
      "v_mfma_f32_16x16x32_bf16 v[252:255], v[84:87], v[144:147], v[252:255]\n"
      "ds_write_b128 v0, v[224:227] offset:0\n"
      "v_mfma_f32_16x16x32_bf16 v[240:243], v[88:91], v[148:151], v[240:243]\n"
      "v_mfma_f32_16x16x32_bf16 v[244:247], v[92:95], v[148:151], v[244:247]\n"
      "ds_write_b128 v1, v[228:231] offset:1024\n"
      "v_mfma_f32_16x16x32_bf16 v[248:251], v[96:99], v[148:151], v[248:251]\n"
      "v_mfma_f32_16x16x32_bf16 v[252:255], v[100:103], v[148:151], v[252:255]\n"
      "s_waitcnt vmcnt(0)\n"
      "v_mfma_f32_16x16x32_bf16 v[240:243], v[104:107], v[152:155], v[240:243]\n"
      "v_mfma_f32_16x16x32_bf16 v[244:247], v[108:111], v[152:155], v[244:247]\n"
      "v_mfma_f32_16x16x32_bf16 v[248:251], v[112:115], v[152:155], v[248:251]\n"
      "v_mfma_f32_16x16x32_bf16 v[252:255], v[116:119], v[152:155], v[252:255]\n"
      "ds_write_b128 v0, v[232:235] offset:2048\n"
      "v_mfma_f32_16x16x32_bf16 v[240:243], v[120:123], v[156:159], v[240:243]\n"
      "v_mfma_f32_16x16x32_bf16 v[244:247], v[124:127], v[156:159], v[244:247]\n"
      "ds_write_b128 v1, v[236:239] offset:3072\n"
      "v_mfma_f32_16x16x32_bf16 v[248:251], v[128:131], v[156:159], v[248:251]\n"
      "v_mfma_f32_16x16x32_bf16 v[252:255], v[132:135], v[156:159], v[252:255]\n"
      "s_nop 4\n"
      "ds_write_b128 v0, v[240:243] offset:4096\n"
      "ds_write_b128 v1, v[244:247] offset:5120\n"
      "ds_write_b128 v0, v[248:251] offset:6144\n"
      "ds_write_b128 v1, v[252:255] offset:7168\n"
      "s_waitcnt lgkmcnt(0)\n"
      "s_barrier\n"
      "s_endpgm\n"
      ".Lexit:\n"
      "s_endpgm\n"
      ".Lend_k:\n"
      :
      : "s"(__builtin_amdgcn_kernarg_segment_ptr()), "s"(blockIdx.x), "v"(threadIdx.x), "s"((unsigned)(size_t)lds)
      : "s4", "s5", "s6", "s7", "s8", "s9", "s10", "s11", "s12", "s13", "s14", "s15", "s16", "s17", "s18", "s19", "s20", "s21", "s22", "s23", "s24", "s25", "s26", "s27", "s28", "s29", "s30", "s31", "s33", "s34", "s35", "s36", "s37", "s38", "s39", "s40", "s41", "s42", "s43", "s44", "s45", "s46", "s47", "s48", "s49", "s50", "s51", "s52", "s53", "s54", "s55", "s56", "s57", "s58", "s59", "s60", "s61", "s62", "s63", "s64", "s65", "s66", "s67", "s68", "s69", "s70", "s71", "s72", "s73", "s74", "s75", "s76", "s77", "s78", "s79", "s80", "s81", "s82", "s83", "s84", "s85", "s86", "s87", "s88", "s89", "s90", "s91", "s92", "s93", "s94", "s95", "v1", "v2", "v3", "v4", "v5", "v6", "v7", "v8", "v9", "v10", "v11", "v12", "v13", "v14", "v15", "v16", "v17", "v18", "v19", "v20", "v21", "v22", "v23", "v24", "v25", "v26", "v27", "v28", "v29", "v30", "v31", "v32", "v33", "v34", "v35", "v36", "v37", "v38", "v39", "v40", "v41", "v42", "v43", "v44", "v45", "v46", "v47", "v48", "v49", "v50", "v51", "v52", "v53", "v54", "v55", "v56", "v57", "v58", "v59", "v60", "v61", "v62", "v63", "v64", "v65", "v66", "v67", "v68", "v69", "v70", "v71", "v72", "v73", "v74", "v75", "v76", "v77", "v78", "v79", "v80", "v81", "v82", "v83", "v84", "v85", "v86", "v87", "v88", "v89", "v90", "v91", "v92", "v93", "v94", "v95", "v96", "v97", "v98", "v99", "v100", "v101", "v102", "v103", "v104", "v105", "v106", "v107", "v108", "v109", "v110", "v111", "v112", "v113", "v114", "v115", "v116", "v117", "v118", "v119", "v120", "v121", "v122", "v123", "v124", "v125", "v126", "v127", "v128", "v129", "v130", "v131", "v132", "v133", "v134", "v135", "v136", "v137", "v138", "v139", "v140", "v141", "v142", "v143", "v144", "v145", "v146", "v147", "v148", "v149", "v150", "v151", "v152", "v153", "v154", "v155", "v156", "v157", "v158", "v159", "v160", "v161", "v162", "v163", "v164", "v165", "v166", "v167", "v168", "v169", "v170", "v171", "v172", "v173", "v174", "v175", "v176", "v177", "v178", "v179", "v180", "v181", "v182", "v183", "v184", "v185", "v186", "v187", "v188", "v189", "v190", "v191", "v192", "v193", "v194", "v195", "v196", "v197", "v198", "v199", "v200", "v201", "v202", "v203", "v204", "v205", "v206", "v207", "v208", "v209", "v210", "v211", "v212", "v213", "v214", "v215", "v216", "v217", "v218", "v219", "v220", "v221", "v222", "v223", "v224", "v225", "v226", "v227", "v228", "v229", "v230", "v231", "v232", "v233", "v234", "v235", "v236", "v237", "v238", "v239", "v240", "v241", "v242", "v243", "v244", "v245", "v246", "v247", "v248", "v249", "v250", "v251", "v252", "v253", "v254", "v255", "vcc", "scc", "memory");
}

extern "C" void kernel_launch(void* const* d_in, const int* in_sizes, int n_in, void* d_out, int out_size, void* d_ws, size_t ws_size,
                              hipStream_t stream) {
  mixer_mlp<<<256, 512, 0, stream>>>(d_in[0], d_in[1], d_in[2], d_in[3], d_in[4], d_out, d_ws);
}
```
